```python
import torch

def _softmax(x, axis=-1):
    return torch.softmax(x, dim=axis)
def _gelu(x):
    return torch.nn.functional.gelu(x, approximate="tanh")
def _silu(x):
    return torch.nn.functional.silu(x)
def _sigmoid(x):
    return torch.sigmoid(x)
def _layernorm(x, axis=-1, eps=1e-5):
    m = x.mean(dim=axis, keepdim=True)
    v = x.var(dim=axis, keepdim=True, unbiased=False)
    return (x - m) / torch.sqrt(v + eps)
def _scatter_add(tgt, idx, src, axis=0):
    return tgt.clone().index_add_(axis, idx, src)
def _segment_sum(src, seg, n):
    out = torch.zeros((n,) + src.shape[1:], dtype=src.dtype)
    return out.index_add_(0, seg, src)
def _pad(x, pw):
    flat = tuple(v for pair in reversed(pw) for v in pair)
    return torch.nn.functional.pad(x, flat)

kernel_name = 'concat_heads__mha_block_cached__gqa_block_cached__near_ridge'

def setup_inputs():

    _w = (lambda a: (lambda t: t.to(torch.bfloat16) if t.is_floating_point() else t)(torch.as_tensor(a)))
    def _A_setup():
        B, Sq, D = 8, 208, 512
        H, Sk, Dh = 32, 512, 128
        return {
            "x": _w((lambda *s: torch.randn(*s, dtype=torch.bfloat16) * 0.8)(B, Sq, D)),
            "k_cache": _w((lambda *s: torch.randn(*s, dtype=torch.bfloat16) * 0.8)(B, H, Sk, Dh)),
            "v_cache": _w((lambda *s: torch.randn(*s, dtype=torch.bfloat16) * 0.05)(B, H, Sk, Dh)),
            "wq": _w((lambda *s: torch.randn(*s, dtype=torch.bfloat16) * 0.8)(D, H * Dh) * (D ** -0.5)),
            "wo": _w((lambda *s: torch.randn(*s, dtype=torch.bfloat16) * 0.05)(H * Dh, D) * ((H * Dh) ** -0.5)),
        }
    _A_in = _A_setup()

    def _B_setup():
        B, Sq, D = 8, 208, 512
        Hq, Hkv, Sk, Dh = 32, 32, 512, 128
        assert Hq % Hkv == 0, (Hq, Hkv)
        return {
            "x": _w((lambda *s: torch.randn(*s, dtype=torch.bfloat16) * 0.8)(B, Sq, D)),
            "k_cache": _w((lambda *s: torch.randn(*s, dtype=torch.bfloat16) * 0.8)(B, Hkv, Sk, Dh)),
            "v_cache": _w((lambda *s: torch.randn(*s, dtype=torch.bfloat16) * 0.05)(B, Hkv, Sk, Dh)),
            "wq": _w((lambda *s: torch.randn(*s, dtype=torch.bfloat16) * 0.8)(D, Hq * Dh) * (D ** -0.5)),
            "wo": _w((lambda *s: torch.randn(*s, dtype=torch.bfloat16) * 0.05)(Hq * Dh, D) * ((Hq * Dh) ** -0.5)),
        }
    _B_in = _B_setup()

    return {
        "A_x": _A_in["x"],
        "A_k_cache": _A_in["k_cache"],
        "A_v_cache": _A_in["v_cache"],
        "A_wq": _A_in["wq"],
        "A_wo": _A_in["wo"],
        "B_x": _B_in["x"],
        "B_k_cache": _B_in["k_cache"],
        "B_v_cache": _B_in["v_cache"],
        "B_wq": _B_in["wq"],
        "B_wo": _B_in["wo"],
    }

def reference(A_x, A_k_cache, A_v_cache, A_wq, A_wo, B_x, B_k_cache, B_v_cache, B_wq, B_wo):
    def _A_ref(x, k_cache, v_cache, wq, wo):
        B, Sq, D = x.shape
        H, Dh = 32, 128
        q = (lambda x, *d: x.permute(*d))((x @ wq).reshape(B, Sq, H, Dh), 0, 2, 1, 3)
        scores = torch.einsum("bhqd,bhkd->bhqk", q, k_cache) * (Dh ** -0.5)
        p = _softmax(scores, axis=-1)
        o = torch.einsum("bhqk,bhkd->bhqd", p, v_cache)
        return (lambda x, *d: x.permute(*d))(o, 0, 2, 1, 3).reshape(B, Sq, H * Dh) @ wo

    def _B_ref(x, k_cache, v_cache, wq, wo):
        B, Sq, D = x.shape
        Hq, Dh = 32, 128
        Hkv = k_cache.shape[1]
        rep = Hq // Hkv
        q = (lambda x, *d: x.permute(*d))((x @ wq).reshape(B, Sq, Hq, Dh), 0, 2, 1, 3)
        k = (lambda x, n, axis: torch.repeat_interleave(x, n, dim=axis))(k_cache, rep, axis=1)
        v = (lambda x, n, axis: torch.repeat_interleave(x, n, dim=axis))(v_cache, rep, axis=1)
        scores = torch.einsum("bhqd,bhkd->bhqk", q, k) * (Dh ** -0.5)
        p = _softmax(scores, axis=-1)
        o = torch.einsum("bhqk,bhkd->bhqd", p, v)
        return (lambda x, *d: x.permute(*d))(o, 0, 2, 1, 3).reshape(B, Sq, Hq * Dh) @ wo

    a = _A_ref(A_x, A_k_cache, A_v_cache, A_wq, A_wo)
    b = _B_ref(B_x, B_k_cache, B_v_cache, B_wq, B_wo)
    return torch.concatenate([a, b], axis=-1)
```

```cpp
#include <hip/hip_runtime.h>
#include <hip/hip_bf16.h>
#include <cstdio>
#include <cstdint>

typedef short bf16x8 __attribute__((ext_vector_type(8)));
typedef short s16x4 __attribute__((ext_vector_type(4)));
typedef float f32x4 __attribute__((ext_vector_type(4)));
typedef float f32x2 __attribute__((ext_vector_type(2)));
typedef unsigned u32x4 __attribute__((ext_vector_type(4)));
typedef unsigned u32x2 __attribute__((ext_vector_type(2)));
typedef __bf16 bf16x2_t __attribute__((ext_vector_type(2)));
typedef __attribute__((address_space(3))) const char* lds_cptr;
typedef __attribute__((address_space(3))) char* lds_ptr;
typedef short v4i16_t __attribute__((ext_vector_type(4)));

#define DI __device__ __forceinline__
#define MFMA16(a, b, c) __builtin_amdgcn_mfma_f32_16x16x32_bf16((a), (b), (c), 0, 0, 0)
#define SBAR() __builtin_amdgcn_sched_barrier(0)

constexpr int SQ = 208, DM = 512, NH = 32, SK = 512, DH = 128, NB = 8, NQB = 13;
constexpr float QSCALE = 0.12751743082f;

DI unsigned cvtpk(float lo, float hi) {
  f32x2 v = {lo, hi}; bf16x2_t b = __builtin_convertvector(v, bf16x2_t); return __builtin_bit_cast(unsigned, b);
}
DI bf16x8 lds_b128(lds_cptr p) { return *(const __attribute__((address_space(3))) bf16x8*)p; }
DI s16x4 lds_tr(lds_cptr p) { return __builtin_bit_cast(s16x4, __builtin_amdgcn_ds_read_tr16_b64_v4i16((__attribute__((address_space(3))) v4i16_t*)p)); }
DI bf16x8 lds_tr2(lds_cptr p, int off2) { s16x4 a = lds_tr(p), b = lds_tr(p + off2); return __builtin_shufflevector(a, b, 0, 1, 2, 3, 4, 5, 6, 7); }

template <int OFF> DI void glds(unsigned voff, const void* sbase, unsigned m0v) {
  asm volatile("s_mov_b32 m0, %2\n\ts_nop 0\n\tglobal_load_lds_dwordx4 %0, %1 offset:%3" :: "v"(voff), "s"(sbase), "s"(m0v), "i"(OFF) : "memory", "m0");
}
#define WAITV(n) asm volatile("s_waitcnt vmcnt(" #n ")" ::: "memory")
#define WAITVL_BAR(n) asm volatile("s_waitcnt vmcnt(" #n ") lgkmcnt(0)\n\ts_barrier" ::: "memory")
#define WAITL_BAR() asm volatile("s_waitcnt lgkmcnt(0)\n\ts_barrier" ::: "memory")

constexpr int L_W0 = 16384, L_X = 49152, L_X13 = 73728, L_Q13 = 77824, L_ATT = 81920;

__global__ __launch_bounds__(256, 2) void attn_kernel(const char* __restrict__ xA, const char* __restrict__ kA, const char* __restrict__ vA, const char* __restrict__ wqA,
                                                      const char* __restrict__ xB, const char* __restrict__ kB, const char* __restrict__ vB, const char* __restrict__ wqB,
                                                      char* __restrict__ Ows) {
  extern __shared__ __attribute__((aligned(16))) char smem[];
  const unsigned lds0 = (unsigned)(uintptr_t)smem;
  const lds_cptr L = (lds_cptr)smem;
  const int tid = threadIdx.x, lane = tid & 63;
  const int w = __builtin_amdgcn_readfirstlane(tid >> 6);
  const int bid = blockIdx.x, xcd = bid & 7, idx = bid >> 3;
  const int br = xcd >> 2, h = (xcd & 3) * 8 + (idx & 7), b = idx >> 3;
  const int rot = (b + idx) & 7;
  const char* xg = (br ? xB : xA) + (size_t)b * SQ * DM * 2;
  const char* wq = (br ? wqB : wqA) + h * 256;
  const char* kg = (br ? kB : kA) + (size_t)(b * NH + h) * SK * DH * 2;
  const char* vg = (br ? vB : vA) + (size_t)(b * NH + h) * SK * DH * 2;

  const int r8 = (lane >> 2) & 7, st = lane >> 5, c4 = lane & 3;
  const unsigned chk_w = (unsigned)((4 * st + (c4 ^ (2 * (w & 1)))) * 16);
  const unsigned voffKV = (unsigned)((8 * w + r8) * 256) + chk_w;
  const unsigned voffW = (unsigned)((8 * w + r8) * 8192) + chk_w;
  const unsigned voffX0 = (unsigned)(r8 * 1024 + (4 * st + c4) * 16);
  const unsigned voffX1 = (unsigned)((8 + r8) * 1024 + (4 * st + (c4 ^ 2)) * 16);

  auto dma_kv = [&](int t) {
    const unsigned dst = lds0 + (unsigned)(t & 3) * 16384u + (unsigned)w * 2048u;
    const char* ks = kg + t * 8192; const char* vs = vg + t * 8192;
    glds<0>(voffKV, ks, dst); glds<128>(voffKV, ks, dst + 1024 - 128);
    glds<0>(voffKV, vs, dst + 8192); glds<128>(voffKV, vs, dst + 8192 + 1024 - 128);
  };
  auto dma_w = [&](int c) {
    const int kc = (c + rot) & 7;
    const unsigned dst = lds0 + L_W0 + (unsigned)(c & 1) * 16384u + (unsigned)w * 2048u;
    const char* s0 = wq + (size_t)kc * 64 * 8192; const char* s1 = s0 + 32 * 8192;
    glds<0>(voffW, s0, dst); glds<128>(voffW, s0, dst + 1024 - 128);
    glds<0>(voffW, s1, dst + 8192); glds<128>(voffW, s1, dst + 8192 + 1024 - 128);
    if (w < 2) {
      const char* xs = xg + 192 * 1024 + kc * 128;
      glds<0>(w ? voffX1 : voffX0, xs, lds0 + L_X13 + (unsigned)(c & 1) * 2048u + (unsigned)w * 1024u);
    }
  };
  auto dma_x = [&](int c) {
    const int kc = (c + rot) & 7;
    const unsigned dst = lds0 + L_X + (unsigned)w * 6144u;
    const char* xs = xg + (size_t)(w * 48) * 1024 + kc * 128;
#pragma unroll
    for (int qb = 0; qb < 3; ++qb) { glds<0>(voffX0, xs + qb * 16384, dst + qb * 2048); glds<0>(voffX1, xs + qb * 16384, dst + qb * 2048 + 1024); }
  };

  const int q16 = lane & 15, g = lane >> 4, qp = (lane & 15) >> 2, p4 = lane & 3;
  const unsigned xrd = (unsigned)((q16 >> 3) * 1024 + (q16 & 7) * 64 + ((g ^ (2 * (q16 >> 3))) * 16));
  const unsigned trE = (unsigned)(g * 2048 + qp * 64 + ((g & 1) * 32) + 8 * p4);
  const unsigned trO = (unsigned)(g * 2048 + qp * 64 + (((g & 1) ^ 1) * 32) + 8 * p4);
  const unsigned krd = (unsigned)((q16 >> 2) * 2048 + (q16 & 3) * 64 + ((g ^ (2 * ((q16 >> 2) & 1))) * 16));

  dma_w(0); dma_x(0); dma_kv(0);
  f32x4 acc[3][8]; f32x4 acc13[2];
#pragma unroll
  for (int qb = 0; qb < 3; ++qb)
#pragma unroll
    for (int c = 0; c < 8; ++c) acc[qb][c] = f32x4{0, 0, 0, 0};
  acc13[0] = f32x4{0, 0, 0, 0}; acc13[1] = f32x4{0, 0, 0, 0};
  for (int c = 0; c < 8; ++c) {
    if (c == 0) { WAITVL_BAR(4); } else { WAITVL_BAR(0); }
    bf16x8 xf[3][2], xf13[2];
    const lds_cptr xp = L + L_X + w * 6144 + xrd;
#pragma unroll
    for (int qb = 0; qb < 3; ++qb) { xf[qb][0] = lds_b128(xp + qb * 2048); xf[qb][1] = lds_b128(xp + qb * 2048 + 512); }
    const lds_cptr xp13 = L + L_X13 + (c & 1) * 2048 + xrd;
    xf13[0] = lds_b128(xp13); xf13[1] = lds_b128(xp13 + 512);
    asm volatile("s_waitcnt lgkmcnt(0)" : "+v"(xf[0][0]), "+v"(xf[0][1]), "+v"(xf[1][0]), "+v"(xf[1][1]), "+v"(xf[2][0]), "+v"(xf[2][1]) :: "memory");
    if (c < 7) { dma_w(c + 1); dma_x(c + 1); } else { dma_kv(1); }
    const lds_cptr wE = L + L_W0 + (c & 1) * 16384 + trE, wO = L + L_W0 + (c & 1) * 16384 + trO;
#pragma unroll
    for (int ks = 0; ks < 2; ++ks) {
#pragma unroll
      for (int ct = 0; ct < 8; ++ct) {
        const bf16x8 a = lds_tr2(((ct & 1) ? wO : wE) + ks * 8192 + (ct >> 1) * 512, 256);
#pragma unroll
        for (int qb = 0; qb < 3; ++qb) acc[qb][ct] = MFMA16(a, xf[qb][ks], acc[qb][ct]);
      }
      const bf16x8 a0 = lds_tr2(wE + ks * 8192 + w * 512, 256), a1 = lds_tr2(wO + ks * 8192 + w * 512, 256);
      acc13[0] = MFMA16(a0, xf13[ks], acc13[0]); acc13[1] = MFMA16(a1, xf13[ks], acc13[1]);
    }
  }
  WAITL_BAR();
  dma_kv(2); dma_kv(3);
  auto mkq = [&](const f32x4& t0, const f32x4& t1) -> bf16x8 {
    unsigned a0 = cvtpk(t0[0] * QSCALE, t0[1] * QSCALE), a1 = cvtpk(t0[2] * QSCALE, t0[3] * QSCALE);
    unsigned b0 = cvtpk(t1[0] * QSCALE, t1[1] * QSCALE), b1 = cvtpk(t1[2] * QSCALE, t1[3] * QSCALE);
    auto r0 = __builtin_amdgcn_permlane32_swap(a0, b0, false, false); auto r1 = __builtin_amdgcn_permlane32_swap(a1, b1, false, false);
    auto s0 = __builtin_amdgcn_permlane16_swap(r0[0], r0[1], false, false); auto s1 = __builtin_amdgcn_permlane16_swap(r1[0], r1[1], false, false);
    u32x4 o = {s0[0], s1[0], s0[1], s1[1]};
    return __builtin_bit_cast(bf16x8, o);
  };
  bf16x8 Q[3][4];
#pragma unroll
  for (int qb = 0; qb < 3; ++qb)
#pragma unroll
    for (int s = 0; s < 4; ++s) Q[qb][s] = mkq(acc[qb][2 * s], acc[qb][2 * s + 1]);
  { const bf16x8 q13 = mkq(acc13[0], acc13[1]);
    *(__attribute__((address_space(3))) bf16x8*)((lds_ptr)smem + L_Q13 + w * 1024 + lane * 16) = q13; }
  WAITL_BAR();

  f32x4 O[3][8], O13[8];
#pragma unroll
  for (int c = 0; c < 8; ++c) { O[0][c] = f32x4{0, 0, 0, 0}; O[1][c] = f32x4{0, 0, 0, 0}; O[2][c] = f32x4{0, 0, 0, 0}; O13[c] = f32x4{0, 0, 0, 0}; }
  float ls[3] = {0.f, 0.f, 0.f}, l13 = 0.f;
  for (int t = 0; t < 16; ++t) {
    if (t > 0) {
      if (t < 14) { WAITVL_BAR(8); } else if (t == 14) { WAITVL_BAR(4); } else { WAITVL_BAR(0); }
      if (t < 13) dma_kv(t + 3);
    }
    const lds_cptr kp = L + (t & 3) * 16384 + krd;
    const lds_cptr vE = L + (t & 3) * 16384 + 8192 + trE, vO = L + (t & 3) * 16384 + 8192 + trO;
    f32x4 S[3][2];
    bf16x8 f0 = lds_b128(kp), f1 = lds_b128(kp + 512), f2;
    __builtin_amdgcn_s_setprio(1);
#define QKG(tt, s, FC, FN, NEXT) do { FN = NEXT; \
      _Pragma("unroll") for (int qb = 0; qb < 3; ++qb) S[qb][tt] = MFMA16(FC, Q[qb][s], (s) == 0 ? (f32x4{0, 0, 0, 0}) : S[qb][tt]); SBAR(); } while (0)
#define VFR(c) lds_tr2((((c) & 1) ? vO : vE) + ((c) >> 1) * 512, 256)
    QKG(0, 0, f0, f2, lds_b128(kp + 1024));
    QKG(0, 1, f1, f0, lds_b128(kp + 1536));
    QKG(0, 2, f2, f1, lds_b128(kp + 256));
    QKG(0, 3, f0, f2, lds_b128(kp + 256 + 512));
    QKG(1, 0, f1, f0, lds_b128(kp + 256 + 1024));
    QKG(1, 1, f2, f1, lds_b128(kp + 256 + 1536));
    QKG(1, 2, f0, f2, VFR(0));
    QKG(1, 3, f1, f0, VFR(1));
    __builtin_amdgcn_s_setprio(0);
    bf16x8 P[3];
#pragma unroll
    for (int qb = 0; qb < 3; ++qb) {
      float e[8];
#pragma unroll
      for (int j = 0; j < 8; ++j) { e[j] = __builtin_amdgcn_exp2f(S[qb][j >> 2][j & 3]); ls[qb] += e[j]; }
      u32x4 pk = {cvtpk(e[0], e[1]), cvtpk(e[2], e[3]), cvtpk(e[4], e[5]), cvtpk(e[6], e[7])};
      P[qb] = __builtin_bit_cast(bf16x8, pk);
    }
    SBAR();
    __builtin_amdgcn_s_setprio(1);
#define PVG(c, FC, FN, NEXT) do { FN = NEXT; \
      _Pragma("unroll") for (int qb = 0; qb < 3; ++qb) O[qb][c] = MFMA16(FC, P[qb], O[qb][c]); SBAR(); } while (0)
    PVG(0, f2, f1, VFR(2));
    PVG(1, f0, f2, VFR(3));
    PVG(2, f1, f0, VFR(4));
    PVG(3, f2, f1, VFR(5));
    PVG(4, f0, f2, VFR(6));
    PVG(5, f1, f0, VFR(7));
    PVG(6, f2, f1, f1);
    PVG(7, f0, f1, f1);
    __builtin_amdgcn_s_setprio(0);
    if ((t & 3) == w) {
      f32x4 S0 = {0, 0, 0, 0}, S1 = {0, 0, 0, 0};
#pragma unroll
      for (int s = 0; s < 4; ++s) {
        const bf16x8 qf = lds_b128(L + L_Q13 + s * 1024 + lane * 16);
        S0 = MFMA16(lds_b128(kp + s * 512), qf, S0); S1 = MFMA16(lds_b128(kp + s * 512 + 256), qf, S1);
      }
      float e[8];
#pragma unroll
      for (int j = 0; j < 4; ++j) { e[j] = __builtin_amdgcn_exp2f(S0[j]); e[4 + j] = __builtin_amdgcn_exp2f(S1[j]); l13 += e[j] + e[4 + j]; }
      u32x4 pk = {cvtpk(e[0], e[1]), cvtpk(e[2], e[3]), cvtpk(e[4], e[5]), cvtpk(e[6], e[7])};
      const bf16x8 P13 = __builtin_bit_cast(bf16x8, pk);
#pragma unroll
      for (int c = 0; c < 8; ++c) O13[c] = MFMA16(lds_tr2(((c & 1) ? vO : vE) + (c >> 1) * 512, 256), P13, O13[c]);
    }
  }
  char* og = Ows + ((size_t)((br * NB + b) * NQB) * 128 + h * 4) * 1024 + lane * 16;
  WAITL_BAR();
  {
    lds_ptr dst = (lds_ptr)smem + w * 8192 + lane * 16;
#pragma unroll
    for (int c = 0; c < 8; ++c) *(__attribute__((address_space(3))) f32x4*)(dst + c * 1024) = O13[c];
    *(__attribute__((address_space(3))) float*)((lds_ptr)smem + 32768 + w * 256 + lane * 4) = l13;
  }
#pragma unroll
  for (int qb = 0; qb < 3; ++qb) {
    float l = ls[qb];
    l += __shfl_xor(l, 16); l += __shfl_xor(l, 32);
    const float il = __builtin_amdgcn_rcpf(l);
#pragma unroll
    for (int s = 0; s < 4; ++s) {
      const f32x4 a = O[qb][2 * s], c = O[qb][2 * s + 1];
      u32x4 pk = {cvtpk(a[0] * il, a[1] * il), cvtpk(a[2] * il, a[3] * il), cvtpk(c[0] * il, c[1] * il), cvtpk(c[2] * il, c[3] * il)};
      *(u32x4*)(og + ((size_t)(w * 3 + qb) * 128 + s) * 1024) = pk;
    }
  }
  WAITL_BAR();
  {
    f32x4 a = {0, 0, 0, 0}, c = {0, 0, 0, 0}; float l = 0.f;
#pragma unroll
    for (int ww = 0; ww < 4; ++ww) {
      a += *(const __attribute__((address_space(3))) f32x4*)(L + ww * 8192 + (2 * w) * 1024 + lane * 16);
      c += *(const __attribute__((address_space(3))) f32x4*)(L + ww * 8192 + (2 * w + 1) * 1024 + lane * 16);
#pragma unroll
      for (int gg = 0; gg < 4; ++gg) l += *(const __attribute__((address_space(3))) float*)(L + 32768 + ww * 256 + (gg * 16 + q16) * 4);
    }
    const float il = __builtin_amdgcn_rcpf(l);
    u32x4 pk = {cvtpk(a[0] * il, a[1] * il), cvtpk(a[2] * il, a[3] * il), cvtpk(c[0] * il, c[1] * il), cvtpk(c[2] * il, c[3] * il)};
    *(u32x4*)(og + ((size_t)12 * 128 + w) * 1024) = pk;
  }
}

constexpr int OP_PITCH = 160, OP_BUF = 32 * OP_PITCH;
constexpr int L_OP = 114688;

template <int NQ> DI void oproj_body(const char* __restrict__ og, const char* __restrict__ wo, char* __restrict__ outp, char* smem, int w, int lane) {
  const lds_cptr L = (lds_cptr)smem;
  const int q16 = lane & 15, g = lane >> 4, qp = (lane & 15) >> 2, p4 = lane & 3;
  lds_ptr wbuf = (lds_ptr)smem + w * (2 * OP_BUF);
  const unsigned wwr = (unsigned)((lane >> 3) * OP_PITCH + (lane & 7) * 16);
  const unsigned wrd = (unsigned)((4 * g + qp) * OP_PITCH + 8 * p4);
  const char* ob = og + lane * 16;
  const char* wb = wo + (size_t)(lane >> 3) * 1024 + (lane & 7) * 16;
  f32x4 acc[NQ][4];
#pragma unroll
  for (int qb = 0; qb < NQ; ++qb)
#pragma unroll
    for (int nt = 0; nt < 4; ++nt) acc[qb][nt] = f32x4{0, 0, 0, 0};
  bf16x8 bA[NQ], bB[NQ]; u32x4 wA[4], wB[4];
#define LOADS(bq, wq_, i) do { const int ks_ = w + 8 * (i); \
    _Pragma("unroll") for (int j = 0; j < 4; ++j) wq_[j] = *(const u32x4*)(wb + (size_t)(32 * ks_ + 8 * j) * 1024); \
    _Pragma("unroll") for (int qb = 0; qb < NQ; ++qb) bq[qb] = *(const bf16x8*)(ob + (size_t)(qb * 128 + ks_) * 1024); } while (0)
#define STEP(bq, wq_, buf) do { \
    _Pragma("unroll") for (int j = 0; j < 4; ++j) *(__attribute__((address_space(3))) u32x4*)(wbuf + (buf) * OP_BUF + wwr + j * 8 * OP_PITCH) = wq_[j]; \
    _Pragma("unroll") for (int nt = 0; nt < 4; ++nt) { const bf16x8 a = lds_tr2((lds_cptr)wbuf + (buf) * OP_BUF + wrd + nt * 32, 16 * OP_PITCH); \
      _Pragma("unroll") for (int qb = 0; qb < NQ; ++qb) acc[qb][nt] = MFMA16(a, bq[qb], acc[qb][nt]); } } while (0)
  LOADS(bA, wA, 0); LOADS(bB, wB, 1);
  for (int i = 0; i < 14; i += 2) {
    { bf16x8 bT[NQ];
#pragma unroll
      for (int qb = 0; qb < NQ; ++qb) bT[qb] = bA[qb];
      u32x4 wT[4] = {wA[0], wA[1], wA[2], wA[3]};
      LOADS(bA, wA, i + 2); STEP(bT, wT, 0); }
    { bf16x8 bT[NQ];
#pragma unroll
      for (int qb = 0; qb < NQ; ++qb) bT[qb] = bB[qb];
      u32x4 wT[4] = {wB[0], wB[1], wB[2], wB[3]};
      LOADS(bB, wB, i + 3); STEP(bT, wT, 1); }
  }
  STEP(bA, wA, 0); STEP(bB, wB, 1);
#undef LOADS
#undef STEP
#pragma unroll
  for (int rd = 0; rd < 2; ++rd) {
    __syncthreads();
#pragma unroll
    for (int qb = 0; qb < NQ; ++qb)
#pragma unroll
      for (int n2 = 0; n2 < 2; ++n2)
        *(__attribute__((address_space(3))) f32x4*)((lds_ptr)smem + ((qb * 2 + n2) * 8 + w) * 1024 + lane * 16) = acc[qb][rd * 2 + n2];
    __syncthreads();
    if (w < NQ) {
      f32x4 s0 = {0, 0, 0, 0}, s1 = {0, 0, 0, 0};
#pragma unroll
      for (int ww = 0; ww < 8; ++ww) {
        s0 += *(const __attribute__((address_space(3))) f32x4*)(L + ((w * 2 + 0) * 8 + ww) * 1024 + lane * 16);
        s1 += *(const __attribute__((address_space(3))) f32x4*)(L + ((w * 2 + 1) * 8 + ww) * 1024 + lane * 16);
      }
      char* op = outp + (size_t)(w * 16 + q16) * 2048 + rd * 64 + g * 8;
      *(u32x2*)(op) = u32x2{cvtpk(s0[0], s0[1]), cvtpk(s0[2], s0[3])};
      *(u32x2*)(op + 32) = u32x2{cvtpk(s1[0], s1[1]), cvtpk(s1[2], s1[3])};
    }
  }
}

__global__ __launch_bounds__(512, 2) void oproj_kernel(const char* __restrict__ Ows, const char* __restrict__ woA, const char* __restrict__ woB, char* __restrict__ out) {
  extern __shared__ __attribute__((aligned(16))) char smem[];
  const int tid = threadIdx.x, lane = tid & 63;
  const int w = __builtin_amdgcn_readfirstlane(tid >> 6);
  const int bid = blockIdx.x, xcd = bid & 7, idx = bid >> 3;
  const int br = xcd >> 2, b = 2 * (xcd & 3) + (idx >> 4), half = (idx >> 3) & 1, ng = idx & 7;
  const char* og = Ows + (size_t)((br * NB + b) * NQB + half * 7) * 128 * 1024;
  const char* wo = (br ? woB : woA) + ng * 128;
  char* outp = out + (size_t)(b * SQ + half * 112) * 2048 + br * 1024 + ng * 128;
  if (half == 0) oproj_body<7>(og, wo, outp, smem, w, lane); else oproj_body<6>(og, wo, outp, smem, w, lane);
}

extern "C" void kernel_launch(void* const* d_in, const int* in_sizes, int n_in, void* d_out, int out_size, void* d_ws, size_t ws_size, hipStream_t stream) {
  (void)hipFuncSetAttribute((const void*)attn_kernel, hipFuncAttributeMaxDynamicSharedMemorySize, L_ATT);
  (void)hipFuncSetAttribute((const void*)oproj_kernel, hipFuncAttributeMaxDynamicSharedMemorySize, L_OP);
  char* Ows = (char*)d_ws;
  attn_kernel<<<512, 256, L_ATT, stream>>>((const char*)d_in[0], (const char*)d_in[1], (const char*)d_in[2], (const char*)d_in[3],
                                           (const char*)d_in[5], (const char*)d_in[6], (const char*)d_in[7], (const char*)d_in[8], Ows);
  oproj_kernel<<<256, 512, L_OP, stream>>>(Ows, (const char*)d_in[4], (const char*)d_in[9], (char*)d_out);
}
```

```cpp
#include <hip/hip_runtime.h>
#include <hip/hip_bf16.h>
#include <cstdint>

typedef short bf16x8 __attribute__((ext_vector_type(8)));
typedef short s16x4 __attribute__((ext_vector_type(4)));
typedef float f32x4 __attribute__((ext_vector_type(4)));
typedef float f32x2 __attribute__((ext_vector_type(2)));
typedef unsigned u32x4 __attribute__((ext_vector_type(4)));
typedef unsigned u32x2 __attribute__((ext_vector_type(2)));
typedef __bf16 bf16x2_t __attribute__((ext_vector_type(2)));
typedef __attribute__((address_space(3))) const char* lds_cptr;
typedef __attribute__((address_space(3))) char* lds_ptr;
typedef short v4i16_t __attribute__((ext_vector_type(4)));

#define DI __device__ __forceinline__
#define MFMA16(a, b, c) __builtin_amdgcn_mfma_f32_16x16x32_bf16((a), (b), (c), 0, 0, 0)
#define SBAR() __builtin_amdgcn_sched_barrier(0)
#define PIN(x) asm volatile("" : "+v"(x))
#define PIN2(a, b) asm volatile("" : "+v"(a), "+v"(b))
#define PIN3(a, b, c) asm volatile("" : "+v"(a), "+v"(b), "+v"(c))

#define POLLSLEEP 4
#define FORCE_SLOW 0
constexpr int SQ = 208, DM = 512, NH = 32, SK = 512, DH = 128, NB = 8, NQB = 13;
constexpr float QSCALE = 0.12751743082f;
constexpr unsigned WS_FLAGS = 0, WS_EPOCH = 4096, WS_O = 8192, O_HEAD_BYTES = NQB * 4096, O_BATCH_BYTES = NH * O_HEAD_BYTES;

DI unsigned cvtpk(float lo, float hi) {
  f32x2 v = {lo, hi}; bf16x2_t b = __builtin_convertvector(v, bf16x2_t); return __builtin_bit_cast(unsigned, b);
}
DI bf16x8 lds_b128(lds_cptr p) { return *(const __attribute__((address_space(3))) bf16x8*)p; }
DI s16x4 lds_tr(lds_cptr p) { return __builtin_bit_cast(s16x4, __builtin_amdgcn_ds_read_tr16_b64_v4i16((__attribute__((address_space(3))) v4i16_t*)p)); }
DI bf16x8 lds_trp(lds_cptr p0, lds_cptr p1) { s16x4 a = lds_tr(p0), b = lds_tr(p1); return __builtin_shufflevector(a, b, 0, 1, 2, 3, 4, 5, 6, 7); }
DI bf16x8 lds_tr2(lds_cptr p, int off2) { return lds_trp(p, p + off2); }
DI unsigned lane_id() { unsigned l; asm volatile("v_mbcnt_lo_u32_b32 %0, -1, 0\n\tv_mbcnt_hi_u32_b32 %0, -1, %0" : "=v"(l)); return l; }

template <int OFF> DI void glds(unsigned voff, const void* sbase, unsigned m0v) {
  asm volatile("s_mov_b32 m0, %2\n\ts_nop 0\n\tglobal_load_lds_dwordx4 %0, %1 offset:%3" :: "v"(voff), "s"(sbase), "s"(m0v), "i"(OFF) : "memory", "m0");
}
template <int OFF> DI void glds_nt(unsigned voff, const void* sbase, unsigned m0v) {
  asm volatile("s_mov_b32 m0, %2\n\ts_nop 0\n\tglobal_load_lds_dwordx4 %0, %1 offset:%3 nt" :: "v"(voff), "s"(sbase), "s"(m0v), "i"(OFF) : "memory", "m0");
}
DI void glds_masked(unsigned voff, const void* sbase, unsigned m0v, unsigned long long mask) {
  asm volatile("s_mov_b32 m0, %2\n\ts_mov_b64 exec, %3\n\tglobal_load_lds_dwordx4 %0, %1\n\ts_mov_b64 exec, -1" :: "v"(voff), "s"(sbase), "s"(m0v), "s"(mask) : "memory", "m0");
}
#define WAITV(n) asm volatile("s_waitcnt vmcnt(" #n ")" ::: "memory")
#define WAITVL_BAR(n) asm volatile("s_waitcnt vmcnt(" #n ") lgkmcnt(0)\n\ts_barrier" ::: "memory")
#define WAITL_BAR() asm volatile("s_waitcnt lgkmcnt(0)\n\ts_barrier" ::: "memory")
#define BAR() asm volatile("s_barrier" ::: "memory")
#define WAITV_BAR(n) asm volatile("s_waitcnt vmcnt(" #n ")\n\ts_barrier" ::: "memory")

constexpr int L_X = 49152, L_X13 = 73728, L_Q13 = 65536, L_P13 = 69632, L_L13 = 71680, L_DUMP = 77824, L_ATT = 81920;
__host__ __device__ constexpr int kslot(int i) { return i == 0 ? 0 : i == 1 ? 16384 : i == 2 ? 24576 : 49152; }
__host__ __device__ constexpr int vslot(int i) { return i == 0 ? 8192 : i == 1 ? 32768 : i == 2 ? 40960 : 57344; }

struct AS {
  f32x4 O[3][8], O13[2], Sa[3], Sb[3];
  bf16x8 Q[3][4], F0, F1, F2, F3, q0, q1;
  float ls[3], l13;
  unsigned act0, act1;
  lds_cptr kpA, kpB, vE1, vE2, vO1, vO2, vE13, vO13, q13p;
  lds_ptr p13w, p13w1;
  const char *kg, *vg;
  unsigned voffKV, dmab;
  int tlim;
};

#define KFR(slot, tt, s) lds_b128(((tt) ? s_.kpB : s_.kpA) + (slot) + (s) * 512)
#define VFR(slot, c) lds_trp((((c) & 1) ? s_.vO1 : s_.vE1) + (slot) + ((c) >> 1) * 512, (((c) & 1) ? s_.vO2 : s_.vE2) + (slot) + ((c) >> 1) * 512)
#define DMA_KV(gp, t, slot, p) do { if ((p) == 0) glds_nt<0>(s_.voffKV, (gp) + (t) * 8192, s_.dmab + (slot)); else glds_nt<128>(s_.voffKV, (gp) + (t) * 8192, s_.dmab + (slot) + 1024 - 128); } while (0)
#define SMH(S, P, qb, TT, hh) do { PIN(S[qb]); const float e0_ = __builtin_amdgcn_exp2f(S[qb][2 * (hh)]), e1_ = __builtin_amdgcn_exp2f(S[qb][2 * (hh) + 1]); \
    s_.ls[qb] += e0_; s_.ls[qb] += e1_; P[qb][2 * (TT) + (hh)] = cvtpk(e0_, e1_); } while (0)
#define SMP(P, qb) PIN2(P[qb], s_.ls[qb])

#define MF_ "v_mfma_f32_16x16x32_bf16 "
#define SKIP_(n) "s_cmp_eq_u32 %" #n ", 0\n\ts_cbranch_scc1 1f\n\t"
#define G3SM_(C0, C1, C2) MF_ "%0, %8, %9, " C0 "\n\tv_exp_f32 %4, %13\n\t" MF_ "%1, %8, %10, " C1 "\n\tv_exp_f32 %5, %14\n\t" MF_ "%2, %8, %11, " C2 "\n\tv_add_f32 %6, %6, %4\n\tv_cvt_pk_bf16_f32 %7, %4, %5\n\tv_add_f32 %6, %6, %5\n\t"
template <bool FIRST, bool SM, int X4>
DI void grp(f32x4& a0, f32x4& a1, f32x4& a2, f32x4& a3, const bf16x8& F, const bf16x8& b0, const bf16x8& b1, const bf16x8& b2, const bf16x8& F4, const bf16x8& b3,
            float s0, float s1, float& l, unsigned& pw, unsigned act) {
  float e0, e1;
  if constexpr (FIRST) {
    static_assert(!FIRST || (SM && X4 != 1), "unused form");
    if constexpr (X4 == 2) asm volatile(G3SM_("0", "0", "0") SKIP_(16) MF_ "%3, %15, %12, 0\n1:"
        : "=&v"(a0), "=&v"(a1), "=&v"(a2), "=&v"(a3), "=&v"(e0), "=&v"(e1), "+v"(l), "=&v"(pw) : "v"(F), "v"(b0), "v"(b1), "v"(b2), "v"(b3), "v"(s0), "v"(s1), "v"(F4), "s"(act) : "scc");
    else asm volatile(MF_ "%0, %7, %8, 0\n\tv_exp_f32 %3, %11\n\t" MF_ "%1, %7, %9, 0\n\tv_exp_f32 %4, %12\n\t" MF_ "%2, %7, %10, 0\n\tv_add_f32 %5, %5, %3\n\tv_cvt_pk_bf16_f32 %6, %3, %4\n\tv_add_f32 %5, %5, %4"
        : "=&v"(a0), "=&v"(a1), "=&v"(a2), "=&v"(e0), "=&v"(e1), "+v"(l), "=&v"(pw) : "v"(F), "v"(b0), "v"(b1), "v"(b2), "v"(s0), "v"(s1));
  } else if constexpr (SM) {
    if constexpr (X4 == 2) asm volatile(G3SM_("%0", "%1", "%2") SKIP_(16) MF_ "%3, %15, %12, %3\n1:"
        : "+v"(a0), "+v"(a1), "+v"(a2), "+v"(a3), "=&v"(e0), "=&v"(e1), "+v"(l), "=&v"(pw) : "v"(F), "v"(b0), "v"(b1), "v"(b2), "v"(b3), "v"(s0), "v"(s1), "v"(F4), "s"(act) : "scc");
    else if constexpr (X4 == 1) asm volatile(G3SM_("%0", "%1", "%2") MF_ "%3, %15, %12, %3"
        : "+v"(a0), "+v"(a1), "+v"(a2), "+v"(a3), "=&v"(e0), "=&v"(e1), "+v"(l), "=&v"(pw) : "v"(F), "v"(b0), "v"(b1), "v"(b2), "v"(b3), "v"(s0), "v"(s1), "v"(F4));
    else asm volatile(MF_ "%0, %7, %8, %0\n\tv_exp_f32 %3, %11\n\t" MF_ "%1, %7, %9, %1\n\tv_exp_f32 %4, %12\n\t" MF_ "%2, %7, %10, %2\n\tv_add_f32 %5, %5, %3\n\tv_cvt_pk_bf16_f32 %6, %3, %4\n\tv_add_f32 %5, %5, %4"
        : "+v"(a0), "+v"(a1), "+v"(a2), "=&v"(e0), "=&v"(e1), "+v"(l), "=&v"(pw) : "v"(F), "v"(b0), "v"(b1), "v"(b2), "v"(s0), "v"(s1));
  } else {
    if constexpr (X4 == 2) asm volatile(MF_ "%0, %4, %5, %0\n\t" MF_ "%1, %4, %6, %1\n\t" MF_ "%2, %4, %7, %2\n\t" SKIP_(10) MF_ "%3, %9, %8, %3\n1:"
        : "+v"(a0), "+v"(a1), "+v"(a2), "+v"(a3) : "v"(F), "v"(b0), "v"(b1), "v"(b2), "v"(b3), "v"(F4), "s"(act) : "scc");
    else if constexpr (X4 == 1) asm volatile(MF_ "%0, %4, %5, %0\n\t" MF_ "%1, %4, %6, %1\n\t" MF_ "%2, %4, %7, %2\n\t" MF_ "%3, %9, %8, %3"
        : "+v"(a0), "+v"(a1), "+v"(a2), "+v"(a3) : "v"(F), "v"(b0), "v"(b1), "v"(b2), "v"(b3), "v"(F4));
    else asm volatile(MF_ "%0, %3, %4, %0\n\t" MF_ "%1, %3, %5, %1\n\t" MF_ "%2, %3, %6, %2"
        : "+v"(a0), "+v"(a1), "+v"(a2) : "v"(F), "v"(b0), "v"(b1), "v"(b2));
  }
}
DI void sm4(const f32x4& T, float& l, lds_ptr dst, unsigned act) {
  float a, b, c, d;
  asm volatile(SKIP_(10) "s_nop 1\n\tv_exp_f32 %0, %5\n\tv_exp_f32 %1, %6\n\tv_exp_f32 %2, %7\n\tv_add_f32 %4, %4, %0\n\tv_exp_f32 %3, %8\n\tv_add_f32 %4, %4, %1\n\tv_cvt_pk_bf16_f32 %0, %0, %1\n\tv_add_f32 %4, %4, %2\n\t"
               "v_cvt_pk_bf16_f32 %1, %2, %3\n\tv_add_f32 %4, %4, %3\n\tds_write2_b32 %9, %0, %1 offset0:0 offset1:1\n1:"
               : "=&v"(a), "=&v"(b), "=&v"(c), "=&v"(d), "+v"(l) : "v"(T[0]), "v"(T[1]), "v"(T[2]), "v"(T[3]), "v"(dst), "s"(act) : "scc", "memory");
}
#define BF(x) __builtin_bit_cast(bf16x8, x)
#define GQK(S, X4, F, q, s, SMF, SS, PP, qb, TT, hh) do { unsigned pw_; \
    grp<(s) == 0, SMF, X4>(S[0], S[1], S[2], T, F, s_.Q[0][s], s_.Q[1][s], s_.Q[2][s], F, q, SS[qb][2 * (hh)], SS[qb][2 * (hh) + 1], s_.ls[qb], pw_, act); \
    if constexpr (SMF) PP[qb][2 * (TT) + (hh)] = pw_; } while (0)
#define GPV(c, F, X4, o13, e, SMF, SS, PP, qb, TT, hh) do { unsigned pw_; \
    grp<false, SMF, X4>(s_.O[0][c], s_.O[1][c], s_.O[2][c], s_.O13[o13], F, BF(Pc[0]), BF(Pc[1]), BF(Pc[2]), e, p13, SS[qb][2 * (hh)], SS[qb][2 * (hh) + 1], s_.ls[qb], pw_, 0u); \
    if constexpr (SMF) PP[qb][2 * (TT) + (hh)] = pw_; } while (0)

template <int TM, bool DOQK, bool LOOK, bool DK, bool DV>
DI void att_iter(AS& s_, u32x4 (&Pc)[3], u32x4 (&Pn)[3], int t) {
  constexpr int K1 = kslot((TM + 1) & 3), K2 = kslot((TM + 2) & 3), V0 = vslot(TM);
  constexpr int P13R = L_P13 - L_Q13 + (TM & 1) * 1024;
  const unsigned act = (TM & 1) ? s_.act0 : s_.act1;
  bf16x8 q2, q3, p13, e0, e1; f32x4 T;
  if constexpr (DK) asm volatile("s_cmp_ge_i32 %1, %0\n\ts_cbranch_scc1 1f\n\ts_sleep 10\n1:" :: "s"(s_.tlim), "s"(t) : "scc");
  if constexpr (!DOQK) { s_.F0 = VFR(V0, 0); s_.F1 = VFR(V0, 1); }
  SBAR();
  if constexpr (DOQK) {
    s_.F2 = KFR(K1, 0, 2); q2 = lds_b128(s_.q13p + 2048);
    GQK(s_.Sa, 2, s_.F0, s_.q0, 0, true, s_.Sb, Pc, 1, 1, 1); SBAR();
    s_.F3 = KFR(K1, 0, 3); q3 = lds_b128(s_.q13p + 3072);
    GQK(s_.Sa, 2, s_.F1, s_.q1, 1, true, s_.Sb, Pc, 2, 1, 0); SBAR();
    s_.F0 = VFR(V0, 0);
    GQK(s_.Sa, 2, s_.F2, q2, 2, true, s_.Sb, Pc, 2, 1, 1); SBAR();
    s_.F1 = VFR(V0, 1);
    GQK(s_.Sa, 2, s_.F3, q3, 3, false, s_.Sb, Pc, 0, 0, 0);
    if constexpr (DK) DMA_KV(s_.kg, t + 4, kslot(TM), 0);
    SBAR();
  } else {
    SMH(s_.Sb, Pc, 1, 1, 1); SMH(s_.Sb, Pc, 2, 1, 0); SMH(s_.Sb, Pc, 2, 1, 1); SMP(Pc, 1); SMP(Pc, 2);
    asm volatile("s_nop 1");
    SBAR();
  }
  s_.F2 = VFR(V0, 2);
  GPV(0, s_.F0, 0, 0, e0, false, s_.Sa, Pn, 0, 0, 0);
  if constexpr (DK) DMA_KV(s_.kg, t + 4, kslot(TM), 1);
  if constexpr (DOQK) sm4(T, s_.l13, ((TM + 1) & 1) ? s_.p13w1 : s_.p13w, act);
  SBAR();
  s_.F3 = VFR(V0, 3);
  GPV(1, s_.F1, 0, 0, e0, DOQK, s_.Sa, Pn, 0, 0, 0); SBAR();
  if constexpr (DOQK) s_.F0 = KFR(K1, 1, 0); else s_.F0 = VFR(V0, 4);
  GPV(2, s_.F2, 0, 0, e0, DOQK, s_.Sa, Pn, 0, 0, 1); SBAR();
  if constexpr (DOQK) s_.F1 = KFR(K1, 1, 1); else { s_.F1 = VFR(V0, 5); p13 = lds_b128(s_.q13p + P13R); e0 = lds_tr2(s_.vE13 + V0, 256); }
  GPV(3, s_.F3, 0, 0, e0, DOQK, s_.Sa, Pn, 1, 0, 0); SBAR();
  if constexpr (DOQK) {
    s_.F2 = KFR(K1, 1, 2);
    GQK(s_.Sb, 0, s_.F0, q2, 0, true, s_.Sa, Pn, 1, 0, 1); SBAR();
    s_.F3 = KFR(K1, 1, 3);
    GQK(s_.Sb, 0, s_.F1, q2, 1, true, s_.Sa, Pn, 2, 0, 0); SBAR();
    s_.F0 = VFR(V0, 4); p13 = lds_b128(s_.q13p + P13R); e0 = lds_tr2(s_.vE13 + V0, 256);
    GQK(s_.Sb, 0, s_.F2, q2, 2, true, s_.Sa, Pn, 2, 0, 1); SBAR();
    s_.F1 = VFR(V0, 5);
    GQK(s_.Sb, 0, s_.F3, q2, 3, false, s_.Sa, Pn, 0, 0, 0);
    if constexpr (DV) DMA_KV(s_.vg, t + 3, vslot((TM + 3) & 3), 0);
    SBAR();
  }
  s_.F2 = VFR(V0, 6); e1 = lds_tr2(s_.vO13 + V0, 256);
  GPV(4, s_.F0, 1, 0, e0, false, s_.Sb, Pn, 0, 0, 0);
  if constexpr (DV) DMA_KV(s_.vg, t + 3, vslot((TM + 3) & 3), 1);
  SBAR();
  s_.F3 = VFR(V0, 7);
  GPV(5, s_.F1, 0, 0, e0, DOQK, s_.Sb, Pn, 0, 1, 0); SBAR();
  if constexpr (LOOK) { s_.F0 = KFR(K2, 0, 0); }
  GPV(6, s_.F2, 1, 1, e1, DOQK, s_.Sb, Pn, 0, 1, 1); SBAR();
  if constexpr (LOOK) { s_.q0 = lds_b128(s_.q13p); s_.F1 = KFR(K2, 0, 1); s_.q1 = lds_b128(s_.q13p + 1024); }
  GPV(7, s_.F3, 0, 0, e0, DOQK, s_.Sb, Pn, 1, 1, 0); SBAR();
}

template <bool WITHV> DI void slow_load(AS& s_, int t, unsigned vo0, unsigned vo1, unsigned kdst, unsigned vdst) {
  asm volatile("s_waitcnt lgkmcnt(0)" ::: "memory");
#pragma unroll
  for (int rp = 0; rp < 2; ++rp) {
    const char* ks = s_.kg + t * 8192 + rp * 4096; const char* vs = s_.vg + t * 8192 + rp * 4096;
    glds<0>(vo0, ks, kdst + rp * 4096); glds<128>(vo0, ks, kdst + rp * 4096 + 1024 - 128);
    glds<0>(vo1, ks, kdst + rp * 4096 + 2048); glds<128>(vo1, ks, kdst + rp * 4096 + 3072 - 128);
    if constexpr (WITHV) {
      glds<0>(vo0, vs, vdst + rp * 4096); glds<128>(vo0, vs, vdst + rp * 4096 + 1024 - 128);
      glds<0>(vo1, vs, vdst + rp * 4096 + 2048); glds<128>(vo1, vs, vdst + rp * 4096 + 3072 - 128);
    }
  }
  WAITV(0);
}
DI void slow_scores(AS& s_, f32x4 (&S)[4][2], unsigned kreg) {
#pragma unroll
  for (int s = 0; s < 4; ++s) {
    const bf16x8 ka = KFR(kreg, 0, s), kb = KFR(kreg, 1, s), qf = lds_b128(s_.q13p + s * 1024);
#pragma unroll
    for (int qb = 0; qb < 4; ++qb) {
      const bf16x8 q = qb < 3 ? s_.Q[qb][s] : qf;
      S[qb][0] = MFMA16(ka, q, s == 0 ? (f32x4{0, 0, 0, 0}) : S[qb][0]); S[qb][1] = MFMA16(kb, q, s == 0 ? (f32x4{0, 0, 0, 0}) : S[qb][1]);
      PIN2(S[qb][0], S[qb][1]);
    }
    SBAR();
  }
}
DI void att_slow(AS& s_, float (&l)[4], unsigned lds0, int w) {
  const unsigned ln = lane_id();
  const unsigned kreg = (unsigned)(w < 3 ? w * 16384 : 49152), vreg = (unsigned)(w < 3 ? w * 16384 + 8192 : 73728);
  const unsigned vo0 = ((ln >> 2) & 7) * 256 + (4 * (ln >> 5) + (ln & 3)) * 16, vo1 = (vo0 ^ 32u) + 2048;
  float m[4] = {-INFINITY, -INFINITY, -INFINITY, -INFINITY};
  WAITV(0);
#pragma unroll 1
  for (int t = 0; t < 16; ++t) {
    slow_load<false>(s_, t, vo0, vo1, lds0 + kreg, lds0 + vreg);
    f32x4 S[4][2];
    slow_scores(s_, S, kreg);
#pragma unroll
    for (int qb = 0; qb < 4; ++qb)
#pragma unroll
      for (int j = 0; j < 8; ++j) m[qb] = fmaxf(m[qb], S[qb][j >> 2][j & 3]);
  }
#pragma unroll
  for (int qb = 0; qb < 4; ++qb) { m[qb] = fmaxf(m[qb], __shfl_xor(m[qb], 16)); m[qb] = fmaxf(m[qb], __shfl_xor(m[qb], 32)); l[qb] = 0.f; }
#pragma unroll
  for (int c = 0; c < 8; ++c) { s_.O[0][c] = f32x4{0, 0, 0, 0}; s_.O[1][c] = f32x4{0, 0, 0, 0}; s_.O[2][c] = f32x4{0, 0, 0, 0}; }
  s_.O13[0] = f32x4{0, 0, 0, 0}; s_.O13[1] = f32x4{0, 0, 0, 0};
#pragma unroll 1
  for (int t = 0; t < 16; ++t) {
    slow_load<true>(s_, t, vo0, vo1, lds0 + kreg, lds0 + vreg);
    f32x4 S[4][2];
    slow_scores(s_, S, kreg);
    bf16x8 P[4];
#pragma unroll
    for (int qb = 0; qb < 4; ++qb) {
      float e[8];
#pragma unroll
      for (int j = 0; j < 8; ++j) { e[j] = __builtin_amdgcn_exp2f(S[qb][j >> 2][j & 3] - m[qb]); l[qb] += e[j]; }
      const u32x4 pk = {cvtpk(e[0], e[1]), cvtpk(e[2], e[3]), cvtpk(e[4], e[5]), cvtpk(e[6], e[7])};
      P[qb] = BF(pk);
      PIN2(P[qb], l[qb]); SBAR();
    }
#pragma unroll
    for (int c = 0; c < 8; ++c) {
      const bf16x8 vf = VFR(vreg, c);
#pragma unroll
      for (int qb = 0; qb < 3; ++qb) s_.O[qb][c] = MFMA16(vf, P[qb], s_.O[qb][c]);
      PIN3(s_.O[0][c], s_.O[1][c], s_.O[2][c]); SBAR();
    }
    s_.O13[0] = MFMA16(VFR(vreg + w * 512, 0), P[3], s_.O13[0]);
    s_.O13[1] = MFMA16(VFR(vreg + w * 512, 1), P[3], s_.O13[1]);
    PIN2(s_.O13[0], s_.O13[1]); SBAR();
  }
#pragma unroll
  for (int qb = 0; qb < 4; ++qb) { l[qb] += __shfl_xor(l[qb], 16); l[qb] += __shfl_xor(l[qb], 32); }
}

constexpr int C_SLOT = 4096, C_WAVE = 4 * C_SLOT;
struct CS {
  unsigned ready, tag_lo, tag_hi, fo, voffO, voffW, wdst, sw, hx;
  const char* flags;
  __amdgpu_buffer_rsrc_t srdO, srdW;
  lds_cptr aE, aO;
};
#define c_head(i) (((((i) & 8u) << 1) | ((i) & 7u) | (((i) & 16u) >> 1)) ^ cs.hx)
DI void c_wait_head(CS& c, unsigned h) {
  if (!((c.ready >> h) & 1u)) {
#pragma unroll 1
    for (unsigned spins = 0; spins < (1u << 21); ++spins) {
      u32x2 v;
      asm volatile("global_load_dwordx2 %0, %1, %2 sc1\n\ts_waitcnt vmcnt(0)" : "=v"(v) : "v"(c.fo), "s"(c.flags) : "memory");
      c.ready = (unsigned)__builtin_amdgcn_ballot_w64(v[0] == c.tag_lo && v[1] == c.tag_hi);
      if ((c.ready >> h) & 1u) break;
      __builtin_amdgcn_s_sleep(POLLSLEEP);
    }
  }
}
template <int NQ> struct CT { f32x4 acc[NQ][4]; bf16x8 b[4][NQ]; };
#define C_LDO(dst, soff) asm volatile("buffer_load_dwordx4 %0, %1, %2, %3 offen sc1" : "=v"(dst) : "v"(cs.voffO), "s"(cs.srdO), "s"(soff) : "memory")
#define C_LDW(m0v, soff) asm volatile("s_mov_b32 m0, %2\n\ts_nop 0\n\tbuffer_load_dwordx4 %0, %1, %3 offen lds" :: "v"(cs.voffW), "s"(cs.srdW), "s"(m0v), "s"(soff) : "memory", "m0")
template <int NQ, int SET, bool PRE, int WA, int W0>
DI void c_item(CT<NQ>& ct, CS& cs, unsigned i) {
  unsigned so = 0, swo = 0;
  if constexpr (PRE) { const unsigned hn = c_head(i + 4); c_wait_head(cs, hn); so = hn * O_HEAD_BYTES + cs.sw; swo = hn * 131072u + cs.sw * 32u; }
  asm volatile("s_waitcnt vmcnt(%4)" : "+v"(ct.b[SET][0]), "+v"(ct.b[SET][1]), "+v"(ct.b[SET][2]), "+v"(ct.b[SET][3]) : "i"(WA + W0) : "memory");
  const bf16x8 a0 = lds_tr2(cs.aE + SET * C_SLOT, 2048), a1 = lds_tr2(cs.aO + SET * C_SLOT, 2048), a2 = lds_tr2(cs.aE + SET * C_SLOT + 512, 2048), a3 = lds_tr2(cs.aO + SET * C_SLOT + 512, 2048);
#pragma unroll
  for (int qb = 0; qb < NQ; ++qb) {
    if (qb >= 4) asm volatile("s_waitcnt vmcnt(%1)" : "+v"(ct.b[SET][qb]) : "i"(WA + NQ - 1 - qb + (PRE ? qb + 4 : 0)) : "memory");
    asm volatile(MF_ "%0, %4, %8, %0\n\t" MF_ "%1, %5, %8, %1\n\t" MF_ "%2, %6, %8, %2\n\t" MF_ "%3, %7, %8, %3"
                 : "+v"(ct.acc[qb][0]), "+v"(ct.acc[qb][1]), "+v"(ct.acc[qb][2]), "+v"(ct.acc[qb][3]) : "v"(a0), "v"(a1), "v"(a2), "v"(a3), "v"(ct.b[SET][qb]));
    if constexpr (PRE) {
      C_LDO(ct.b[SET][qb], so + (unsigned)qb * 4096u);
      if (qb < 4) C_LDW(cs.wdst + SET * C_SLOT + qb * 1024, swo + (unsigned)qb * 8192u);
    }
  }
}
template <int NQ> DI void phase_c(CS& cs, char* outp, char* epoch, char* smem, int w) {
  constexpr int NOP = NQ + 4;
  CT<NQ> ct;
#pragma unroll
  for (int s = 0; s < 4; ++s) {
    const unsigned hn = c_head((unsigned)s); c_wait_head(cs, hn); const unsigned so = hn * O_HEAD_BYTES + cs.sw;
#pragma unroll
    for (int qb = 0; qb < NQ; ++qb) C_LDO(ct.b[s][qb], so + (unsigned)qb * 4096u);
  }
#pragma unroll
  for (int qb = 0; qb < NQ; ++qb)
#pragma unroll
    for (int nt = 0; nt < 4; ++nt) ct.acc[qb][nt] = f32x4{0, 0, 0, 0};
  c_item<NQ, 0, true, 3 * NQ, NQ - 4>(ct, cs, 0);
  c_item<NQ, 1, true, 2 * NQ + NOP, NQ - 4>(ct, cs, 1);
  c_item<NQ, 2, true, NQ + 2 * NOP, NQ - 4>(ct, cs, 2);
  c_item<NQ, 3, true, 3 * NOP, NQ - 4>(ct, cs, 3);
  for (unsigned i = 4; i < 28; i += 4) {
    c_item<NQ, 0, true, 3 * NOP, NQ - 4>(ct, cs, i);
    c_item<NQ, 1, true, 3 * NOP, NQ - 4>(ct, cs, i + 1);
    c_item<NQ, 2, true, 3 * NOP, NQ - 4>(ct, cs, i + 2);
    c_item<NQ, 3, true, 3 * NOP, NQ - 4>(ct, cs, i + 3);
  }
  c_item<NQ, 0, false, 3 * NOP, NQ - 4>(ct, cs, 28);
  c_item<NQ, 1, false, 2 * NOP, NQ - 4>(ct, cs, 29);
  c_item<NQ, 2, false, NOP, NQ - 4>(ct, cs, 30);
  c_item<NQ, 3, false, 0, NQ - 4>(ct, cs, 31);
  asm volatile("s_nop 7\n\ts_nop 1");
  SBAR();
  const unsigned lane = lane_id();
  char* op = outp + (size_t)(lane & 15) * 2048 + w * 32 + (lane >> 4) * 8;
#pragma unroll
  for (int rd = 0; rd < 2; ++rd) {
    const int q0 = rd * 4, q1 = rd ? NQ : 4;
    WAITL_BAR();
#pragma unroll
    for (int qb = q0; qb < q1; ++qb)
#pragma unroll
      for (int nt = 0; nt < 4; ++nt)
        *(__attribute__((address_space(3))) f32x4*)((lds_ptr)smem + (((qb - q0) * 4 + nt) * 4 + w) * 1024 + lane * 16) = ct.acc[qb][nt];
    WAITL_BAR();
#pragma unroll
    for (int qb = q0; qb < q1; ++qb) {
      f32x4 s = {0, 0, 0, 0};
#pragma unroll
      for (int ww = 0; ww < 4; ++ww) s += *(const __attribute__((address_space(3))) f32x4*)((lds_cptr)smem + (((qb - q0) * 4 + w) * 4 + ww) * 1024 + lane * 16);
      *(u32x2*)(op + (size_t)qb * 16 * 2048) = u32x2{cvtpk(s[0], s[1]), cvtpk(s[2], s[3])};
    }
  }
  if (epoch && w == 0) {
    const u32x2 tv = {cs.tag_lo, cs.tag_hi}; const unsigned zero = 0u;
    asm volatile("s_mov_b64 exec, 1\n\tglobal_store_dwordx2 %0, %1, %2 sc1\n\ts_mov_b64 exec, -1" :: "v"(zero), "v"(tv), "s"(epoch) : "memory");
  }
  __builtin_amdgcn_endpgm();
}

__global__ __launch_bounds__(256, 2) void mha2_kernel(const char* __restrict__ xA_, const char* __restrict__ kA, const char* __restrict__ vA, const char* __restrict__ wqA, const char* __restrict__ woA,
                                                      const char* __restrict__ xB_, const char* __restrict__ kB, const char* __restrict__ vB, const char* __restrict__ wqB, const char* __restrict__ woB,
                                                      char* ws, char* out) {
  extern __shared__ __attribute__((aligned(16))) char smem[];
  const unsigned lds0 = (unsigned)(uintptr_t)smem;
  const lds_cptr L = (lds_cptr)smem;
  const int tid = threadIdx.x, lane = tid & 63;
  const int w = __builtin_amdgcn_readfirstlane(tid >> 6);
  const int bid = blockIdx.x, xcd = bid & 7, idx = bid >> 3;
  const int br = xcd >> 2, b = ((xcd >> 1) & 1) * 4 + ((idx >> 3) & 3), h = (xcd & 1) * 16 + (idx >> 5) * 8 + (idx & 7);
  if (bid < 256) __builtin_amdgcn_s_setprio(2); else __builtin_amdgcn_s_setprio(1);
  const char* xg = (br ? xB_ : xA_) + (size_t)b * SQ * DM * 2;
  const char* wq = (br ? wqB : wqA) + h * 256;
  AS s_;
  s_.kg = (br ? kB : kA) + (size_t)(b * NH + h) * SK * DH * 2;
  s_.vg = (br ? vB : vA) + (size_t)(b * NH + h) * SK * DH * 2;
  s_.tlim = __builtin_amdgcn_readfirstlane(bid < 256 ? 0 : 10);
  const int r8 = (lane >> 2) & 7, st = lane >> 5, c4 = lane & 3;
  const unsigned chk_w = (unsigned)((4 * st + (c4 ^ (2 * (w & 1)))) * 16);
  s_.voffKV = (unsigned)((8 * w + r8) * 256) + chk_w;
  const unsigned voffW = (unsigned)((8 * w + r8) * 8192) + chk_w;
  const unsigned voffX0 = (unsigned)(r8 * 1024 + (4 * st + c4) * 16);
  const unsigned voffX1 = (unsigned)((8 + r8) * 1024 + (4 * st + (c4 ^ 2)) * 16);
  const unsigned voffX13 = (w & 2) ? voffX1 : voffX0;
  const unsigned long long x13mask = (w & 1) ? 0xFFFFFFFF00000000ull : 0x00000000FFFFFFFFull;
  s_.dmab = lds0 + (unsigned)w * 2048u;
  const unsigned xdst = lds0 + L_X + (unsigned)w * 6144u, x13dst = lds0 + L_X13 + (unsigned)(w >> 1) * 1024u;
  const char* xw = xg + (size_t)(w * 48) * 1024;
  const char* x13 = xg + (size_t)192 * 1024;
#define DMA_W(j, p) do { const char* s0_ = wq + (size_t)(j) * (32 * 8192); \
    if ((p) == 0) glds<0>(voffW, s0_, s_.dmab + 16384 + ((j) & 3) * 8192); else glds<128>(voffW, s0_, s_.dmab + 16384 + ((j) & 3) * 8192 + 1024 - 128); } while (0)
#define DMA_X(c, p) glds<0>(((p) & 1) ? voffX1 : voffX0, xw + ((p) >> 1) * 16384 + (c) * 128, xdst + (p) * 1024)
#define DMA_X13(c) glds_masked(voffX13, x13 + (c) * 128, x13dst + ((c) & 1) * 2048, x13mask)
#define DMA_K(t, slot, p) DMA_KV(s_.kg, t, slot, p)
#define DMA_V(t, slot, p) DMA_KV(s_.vg, t, slot, p)

  const int q16 = lane & 15, g = lane >> 4, qp = (lane & 15) >> 2, p4 = lane & 3;
  const int xro = (q16 >> 3) * 1024 + (q16 & 7) * 64 + ((g ^ (2 * (q16 >> 3))) * 16);
  const lds_cptr xp = L + L_X + w * 6144 + xro, xp13 = L + L_X13 + xro;
  const lds_cptr vE = L + (g * 2048 + qp * 64 + ((g & 1) * 32) + 8 * p4);
  const lds_cptr vO = L + (g * 2048 + qp * 64 + (((g & 1) ^ 1) * 32) + 8 * p4);
  s_.vE13 = vE + w * 512; s_.vO13 = vO + w * 512;

  f32x4 acc[3][8], acc13[2];
  bf16x8 xA[4][2], xB[4][2], F0, F1, F2, F3;
#define A_INIT() do { _Pragma("unroll") for (int qb = 0; qb < 3; ++qb) _Pragma("unroll") for (int c = 0; c < 8; ++c) acc[qb][c] = f32x4{0, 0, 0, 0}; \
    acc13[0] = f32x4{0, 0, 0, 0}; acc13[1] = f32x4{0, 0, 0, 0}; } while (0)
#define WFR(ct, base) lds_tr2((((ct) & 1) ? vO : vE) + (base) + ((ct) >> 1) * 512, 256)
#define WFR_OWN(o, base) lds_tr2(((o) ? s_.vO13 : s_.vE13) + (base), 256)
#define XRD(X, u, sl) X[(u) >> 1][(u) & 1] = lds_b128(((u) < 6 ? xp + ((u) >> 1) * 2048 : xp13 + (sl) * 2048) + ((u) & 1) * 512)
#define XPIN(X) asm volatile("" : "+v"(X[0][0]), "+v"(X[0][1]), "+v"(X[1][0]), "+v"(X[1][1]), "+v"(X[2][0]), "+v"(X[2][1]), "+v"(X[3][0]), "+v"(X[3][1]))
#define MMA(ct, F, X, ks) do { PIN(F); _Pragma("unroll") for (int qb = 0; qb < 3; ++qb) acc[qb][ct] = MFMA16(F, X[qb][ks], acc[qb][ct]); PIN3(acc[0][ct], acc[1][ct], acc[2][ct]); } while (0)
#define MM13(o, F, X, ks) do { PIN(F); acc13[o] = MFMA16(F, X[3][ks], acc13[o]); PIN(acc13[o]); } while (0)
  unsigned long long tag;
  u32x2 ev_;
  { const unsigned zero = 0u; const char* ep = ws + WS_EPOCH + (br * NB + b) * 256;
    asm volatile("global_load_dwordx2 %0, %1, %2 sc1" : "=v"(ev_) : "v"(zero), "s"(ep) : "memory"); }
#define EPOCH_READ() do { PIN(ev_); tag = (((unsigned long long)__builtin_amdgcn_readfirstlane(ev_[1]) << 32) | (unsigned)__builtin_amdgcn_readfirstlane(ev_[0])) + 0x9E3779B97F4A7C15ull; } while (0)
  DMA_W(0, 0);
  DMA_W(0, 1);
  DMA_X(0, 0);
  DMA_X(0, 1);
  DMA_X(0, 2);
  DMA_X(0, 3);
  DMA_X(0, 4);
  DMA_X(0, 5);
  DMA_X13(0);
  DMA_W(1, 0);
  DMA_W(1, 1);
  DMA_W(2, 0);
  DMA_W(2, 1);
  DMA_X13(1);
  A_INIT();
  WAITV_BAR(3);
  EPOCH_READ();
  XRD(xA, 0, 0);
  XRD(xA, 1, 0);
  XRD(xA, 2, 0);
  XRD(xA, 3, 0);
  XRD(xA, 4, 0);
  XRD(xA, 5, 0);
  XRD(xA, 6, 0);
  XRD(xA, 7, 0);
  F0 = WFR(0, 16384);
  F1 = WFR(1, 16384);
  F2 = WFR(2, 16384);
  SBAR();
  F3 = WFR(3, 16384);
  DMA_W(3, 0);
  MMA(0, F0, xA, 0);
  SBAR();
  F0 = WFR_OWN(0, 16384);
  DMA_W(3, 1);
  MMA(1, F1, xA, 0);
  XPIN(xA);
  SBAR();
  F1 = WFR(4, 16384);
  DMA_X(1, 0);
  MMA(2, F2, xA, 0);
  SBAR();
  F2 = WFR(5, 16384);
  DMA_X(1, 1);
  MMA(3, F3, xA, 0);
  SBAR();
  F3 = WFR(6, 16384);
  MM13(0, F0, xA, 0);
  SBAR();
  F0 = WFR(7, 16384);
  DMA_X(1, 2);
  MMA(4, F1, xA, 0);
  SBAR();
  F1 = WFR_OWN(1, 16384);
  DMA_X(1, 3);
  MMA(5, F2, xA, 0);
  SBAR();
  F2 = WFR(0, 24576);
  DMA_X(1, 4);
  MMA(6, F3, xA, 0);
  SBAR();
  F3 = WFR(1, 24576);
  DMA_X(1, 5);
  MMA(7, F0, xA, 0);
  SBAR();
  F0 = WFR(2, 24576);
  MM13(1, F1, xA, 0);
  SBAR();
  WAITV_BAR(8);
  F1 = WFR(3, 24576);
  DMA_W(4, 0);
  MMA(0, F2, xA, 1);
  SBAR();
  F2 = WFR_OWN(0, 24576);
  DMA_W(4, 1);
  MMA(1, F3, xA, 1);
  SBAR();
  F3 = WFR(4, 24576);
  DMA_X13(2);
  MMA(2, F0, xA, 1);
  SBAR();
  F0 = WFR(5, 24576);
  MMA(3, F1, xA, 1);
  SBAR();
  F1 = WFR(6, 24576);
  MM13(0, F2, xA, 1);
  SBAR();
  F2 = WFR(7, 24576);
  MMA(4, F3, xA, 1);
  SBAR();
  F3 = WFR_OWN(1, 24576);
  MMA(5, F0, xA, 1);
  SBAR();
  F0 = WFR(0, 32768);
  WAITV(3);
  XRD(xB, 0, 1);
  XRD(xB, 1, 1);
  XRD(xB, 2, 1);
  XRD(xB, 3, 1);
  MMA(6, F1, xA, 1);
  SBAR();
  F1 = WFR(1, 32768);
  XRD(xB, 4, 1);
  XRD(xB, 5, 1);
  XRD(xB, 6, 1);
  XRD(xB, 7, 1);
  MMA(7, F2, xA, 1);
  SBAR();
  F2 = WFR(2, 32768);
  MM13(1, F3, xA, 1);
  SBAR();
  WAITV_BAR(9);
  F3 = WFR(3, 32768);
  DMA_W(5, 0);
  MMA(0, F0, xB, 0);
  SBAR();
  F0 = WFR_OWN(0, 32768);
  DMA_W(5, 1);
  MMA(1, F1, xB, 0);
  XPIN(xB);
  SBAR();
  F1 = WFR(4, 32768);
  DMA_X(2, 0);
  MMA(2, F2, xB, 0);
  SBAR();
  F2 = WFR(5, 32768);
  DMA_X(2, 1);
  MMA(3, F3, xB, 0);
  SBAR();
  F3 = WFR(6, 32768);
  MM13(0, F0, xB, 0);
  SBAR();
  F0 = WFR(7, 32768);
  DMA_X(2, 2);
  MMA(4, F1, xB, 0);
  SBAR();
  F1 = WFR_OWN(1, 32768);
  DMA_X(2, 3);
  MMA(5, F2, xB, 0);
  SBAR();
  F2 = WFR(0, 40960);
  DMA_X(2, 4);
  MMA(6, F3, xB, 0);
  SBAR();
  F3 = WFR(1, 40960);
  DMA_X(2, 5);
  MMA(7, F0, xB, 0);
  SBAR();
  F0 = WFR(2, 40960);
  MM13(1, F1, xB, 0);
  SBAR();
  WAITV_BAR(8);
  F1 = WFR(3, 40960);
  DMA_W(6, 0);
  MMA(0, F2, xB, 1);
  SBAR();
  F2 = WFR_OWN(0, 40960);
  DMA_W(6, 1);
  MMA(1, F3, xB, 1);
  SBAR();
  F3 = WFR(4, 40960);
  DMA_X13(3);
  MMA(2, F0, xB, 1);
  SBAR();
  F0 = WFR(5, 40960);
  MMA(3, F1, xB, 1);
  SBAR();
  F1 = WFR(6, 40960);
  MM13(0, F2, xB, 1);
  SBAR();
  F2 = WFR(7, 40960);
  MMA(4, F3, xB, 1);
  SBAR();
  F3 = WFR_OWN(1, 40960);
  MMA(5, F0, xB, 1);
  SBAR();
  F0 = WFR(0, 16384);
  WAITV(3);
  XRD(xA, 0, 0);
  XRD(xA, 1, 0);
  XRD(xA, 2, 0);
  XRD(xA, 3, 0);
  MMA(6, F1, xB, 1);
  SBAR();
  F1 = WFR(1, 16384);
  XRD(xA, 4, 0);
  XRD(xA, 5, 0);
  XRD(xA, 6, 0);
  XRD(xA, 7, 0);
  MMA(7, F2, xB, 1);
  SBAR();
  F2 = WFR(2, 16384);
  MM13(1, F3, xB, 1);
  SBAR();
  WAITV_BAR(9);
  F3 = WFR(3, 16384);
  DMA_W(7, 0);
  MMA(0, F0, xA, 0);
  SBAR();
  F0 = WFR_OWN(0, 16384);
  DMA_W(7, 1);
  MMA(1, F1, xA, 0);
  XPIN(xA);
  SBAR();
  F1 = WFR(4, 16384);
  DMA_X(3, 0);
  MMA(2, F2, xA, 0);
  SBAR();
  F2 = WFR(5, 16384);
  DMA_X(3, 1);
  MMA(3, F3, xA, 0);
  SBAR();
  F3 = WFR(6, 16384);
  MM13(0, F0, xA, 0);
  SBAR();
  F0 = WFR(7, 16384);
  DMA_X(3, 2);
  MMA(4, F1, xA, 0);
  SBAR();
  F1 = WFR_OWN(1, 16384);
  DMA_X(3, 3);
  MMA(5, F2, xA, 0);
  SBAR();
  F2 = WFR(0, 24576);
  DMA_X(3, 4);
  MMA(6, F3, xA, 0);
  SBAR();
  F3 = WFR(1, 24576);
  DMA_X(3, 5);
  MMA(7, F0, xA, 0);
  SBAR();
  F0 = WFR(2, 24576);
  MM13(1, F1, xA, 0);
  SBAR();
  WAITV_BAR(8);
  F1 = WFR(3, 24576);
  DMA_W(8, 0);
  MMA(0, F2, xA, 1);
  SBAR();
  F2 = WFR_OWN(0, 24576);
  DMA_W(8, 1);
  MMA(1, F3, xA, 1);
  SBAR();
  F3 = WFR(4, 24576);
  DMA_X13(4);
  MMA(2, F0, xA, 1);
  SBAR();
  F0 = WFR(5, 24576);
  MMA(3, F1, xA, 1);
  SBAR();
  F1 = WFR(6, 24576);
  MM13(0, F2, xA, 1);
  SBAR();
  F2 = WFR(7, 24576);
  MMA(4, F3, xA, 1);
  SBAR();
  F3 = WFR_OWN(1, 24576);
  MMA(5, F0, xA, 1);
  SBAR();
  F0 = WFR(0, 32768);
  WAITV(3);
  XRD(xB, 0, 1);
  XRD(xB, 1, 1);
  XRD(xB, 2, 1);
  XRD(xB, 3, 1);
  MMA(6, F1, xA, 1);
  SBAR();
  F1 = WFR(1, 32768);
  XRD(xB, 4, 1);
  XRD(xB, 5, 1);
  XRD(xB, 6, 1);
  XRD(xB, 7, 1);
  MMA(7, F2, xA, 1);
  SBAR();
  F2 = WFR(2, 32768);
  MM13(1, F3, xA, 1);
  SBAR();
  WAITV_BAR(9);
  F3 = WFR(3, 32768);
  DMA_W(9, 0);
  MMA(0, F0, xB, 0);
  SBAR();
  F0 = WFR_OWN(0, 32768);
  DMA_W(9, 1);
  MMA(1, F1, xB, 0);
  XPIN(xB);
  SBAR();
  F1 = WFR(4, 32768);
  DMA_X(4, 0);
  MMA(2, F2, xB, 0);
  SBAR();
  F2 = WFR(5, 32768);
  DMA_X(4, 1);
  MMA(3, F3, xB, 0);
  SBAR();
  F3 = WFR(6, 32768);
  MM13(0, F0, xB, 0);
  SBAR();
  F0 = WFR(7, 32768);
  DMA_X(4, 2);
  MMA(4, F1, xB, 0);
  SBAR();
  F1 = WFR_OWN(1, 32768);
  DMA_X(4, 3);
  MMA(5, F2, xB, 0);
  SBAR();
  F2 = WFR(0, 40960);
  DMA_X(4, 4);
  MMA(6, F3, xB, 0);
  SBAR();
  F3 = WFR(1, 40960);
  DMA_X(4, 5);
  MMA(7, F0, xB, 0);
  SBAR();
  F0 = WFR(2, 40960);
  MM13(1, F1, xB, 0);
  SBAR();
  WAITV_BAR(8);
  F1 = WFR(3, 40960);
  DMA_W(10, 0);
  MMA(0, F2, xB, 1);
  SBAR();
  F2 = WFR_OWN(0, 40960);
  DMA_W(10, 1);
  MMA(1, F3, xB, 1);
  SBAR();
  F3 = WFR(4, 40960);
  DMA_X13(5);
  MMA(2, F0, xB, 1);
  SBAR();
  F0 = WFR(5, 40960);
  DMA_K(0, 0, 0);
  MMA(3, F1, xB, 1);
  SBAR();
  F1 = WFR(6, 40960);
  MM13(0, F2, xB, 1);
  SBAR();
  F2 = WFR(7, 40960);
  DMA_K(0, 0, 1);
  MMA(4, F3, xB, 1);
  SBAR();
  F3 = WFR_OWN(1, 40960);
  MMA(5, F0, xB, 1);
  SBAR();
  F0 = WFR(0, 16384);
  WAITV(5);
  XRD(xA, 0, 0);
  XRD(xA, 1, 0);
  XRD(xA, 2, 0);
  XRD(xA, 3, 0);
  MMA(6, F1, xB, 1);
  SBAR();
  F1 = WFR(1, 16384);
  XRD(xA, 4, 0);
  XRD(xA, 5, 0);
  XRD(xA, 6, 0);
  XRD(xA, 7, 0);
  MMA(7, F2, xB, 1);
  SBAR();
  F2 = WFR(2, 16384);
  MM13(1, F3, xB, 1);
  SBAR();
  WAITV_BAR(11);
  F3 = WFR(3, 16384);
  DMA_W(11, 0);
  MMA(0, F0, xA, 0);
  SBAR();
  F0 = WFR_OWN(0, 16384);
  DMA_W(11, 1);
  MMA(1, F1, xA, 0);
  XPIN(xA);
  SBAR();
  F1 = WFR(4, 16384);
  DMA_X(5, 0);
  MMA(2, F2, xA, 0);
  SBAR();
  F2 = WFR(5, 16384);
  DMA_X(5, 1);
  MMA(3, F3, xA, 0);
  SBAR();
  F3 = WFR(6, 16384);
  MM13(0, F0, xA, 0);
  SBAR();
  F0 = WFR(7, 16384);
  DMA_X(5, 2);
  MMA(4, F1, xA, 0);
  SBAR();
  F1 = WFR_OWN(1, 16384);
  DMA_X(5, 3);
  MMA(5, F2, xA, 0);
  SBAR();
  F2 = WFR(0, 24576);
  DMA_X(5, 4);
  MMA(6, F3, xA, 0);
  SBAR();
  F3 = WFR(1, 24576);
  DMA_X(5, 5);
  MMA(7, F0, xA, 0);
  SBAR();
  F0 = WFR(2, 24576);
  MM13(1, F1, xA, 0);
  SBAR();
  WAITV_BAR(10);
  F1 = WFR(3, 24576);
  DMA_W(12, 0);
  MMA(0, F2, xA, 1);
  SBAR();
  F2 = WFR_OWN(0, 24576);
  DMA_W(12, 1);
  MMA(1, F3, xA, 1);
  SBAR();
  F3 = WFR(4, 24576);
  DMA_X13(6);
  MMA(2, F0, xA, 1);
  SBAR();
  F0 = WFR(5, 24576);
  DMA_V(0, 8192, 0);
  MMA(3, F1, xA, 1);
  SBAR();
  F1 = WFR(6, 24576);
  MM13(0, F2, xA, 1);
  SBAR();
  F2 = WFR(7, 24576);
  DMA_V(0, 8192, 1);
  MMA(4, F3, xA, 1);
  SBAR();
  F3 = WFR_OWN(1, 24576);
  MMA(5, F0, xA, 1);
  SBAR();
  F0 = WFR(0, 32768);
  WAITV(5);
  XRD(xB, 0, 1);
  XRD(xB, 1, 1);
  XRD(xB, 2, 1);
  XRD(xB, 3, 1);
  MMA(6, F1, xA, 1);
  SBAR();
  F1 = WFR(1, 32768);
  XRD(xB, 4, 1);
  XRD(xB, 5, 1);
  XRD(xB, 6, 1);
  XRD(xB, 7, 1);
  MMA(7, F2, xA, 1);
  SBAR();
  F2 = WFR(2, 32768);
  MM13(1, F3, xA, 1);
  SBAR();
  WAITV_BAR(11);
  F3 = WFR(3, 32768);
  DMA_W(13, 0);
  MMA(0, F0, xB, 0);
  SBAR();
  F0 = WFR_OWN(0, 32768);
  DMA_W(13, 1);
  MMA(1, F1, xB, 0);
  XPIN(xB);
  SBAR();
  F1 = WFR(4, 32768);
  DMA_X(6, 0);
  MMA(2, F2, xB, 0);
  SBAR();
  F2 = WFR(5, 32768);
  DMA_X(6, 1);
  MMA(3, F3, xB, 0);
  SBAR();
  F3 = WFR(6, 32768);
  MM13(0, F0, xB, 0);
  SBAR();
  F0 = WFR(7, 32768);
  DMA_X(6, 2);
  MMA(4, F1, xB, 0);
  SBAR();
  F1 = WFR_OWN(1, 32768);
  DMA_X(6, 3);
  MMA(5, F2, xB, 0);
  SBAR();
  F2 = WFR(0, 40960);
  DMA_X(6, 4);
  MMA(6, F3, xB, 0);
  SBAR();
  F3 = WFR(1, 40960);
  DMA_X(6, 5);
  MMA(7, F0, xB, 0);
  SBAR();
  F0 = WFR(2, 40960);
  MM13(1, F1, xB, 0);
  SBAR();
  WAITV_BAR(10);
  F1 = WFR(3, 40960);
  DMA_W(14, 0);
  MMA(0, F2, xB, 1);
  SBAR();
  F2 = WFR_OWN(0, 40960);
  DMA_W(14, 1);
  MMA(1, F3, xB, 1);
  SBAR();
  F3 = WFR(4, 40960);
  DMA_X13(7);
  MMA(2, F0, xB, 1);
  SBAR();
  F0 = WFR(5, 40960);
  MMA(3, F1, xB, 1);
  SBAR();
  F1 = WFR(6, 40960);
  MM13(0, F2, xB, 1);
  SBAR();
  F2 = WFR(7, 40960);
  MMA(4, F3, xB, 1);
  SBAR();
  F3 = WFR_OWN(1, 40960);
  MMA(5, F0, xB, 1);
  SBAR();
  F0 = WFR(0, 16384);
  WAITV(3);
  XRD(xA, 0, 0);
  XRD(xA, 1, 0);
  XRD(xA, 2, 0);
  XRD(xA, 3, 0);
  MMA(6, F1, xB, 1);
  SBAR();
  F1 = WFR(1, 16384);
  XRD(xA, 4, 0);
  XRD(xA, 5, 0);
  XRD(xA, 6, 0);
  XRD(xA, 7, 0);
  MMA(7, F2, xB, 1);
  SBAR();
  F2 = WFR(2, 16384);
  MM13(1, F3, xB, 1);
  SBAR();
  WAITV_BAR(9);
  F3 = WFR(3, 16384);
  DMA_W(15, 0);
  MMA(0, F0, xA, 0);
  SBAR();
  F0 = WFR_OWN(0, 16384);
  DMA_W(15, 1);
  MMA(1, F1, xA, 0);
  XPIN(xA);
  SBAR();
  F1 = WFR(4, 16384);
  DMA_X(7, 0);
  MMA(2, F2, xA, 0);
  SBAR();
  F2 = WFR(5, 16384);
  DMA_X(7, 1);
  MMA(3, F3, xA, 0);
  SBAR();
  F3 = WFR(6, 16384);
  MM13(0, F0, xA, 0);
  SBAR();
  F0 = WFR(7, 16384);
  DMA_X(7, 2);
  MMA(4, F1, xA, 0);
  SBAR();
  F1 = WFR_OWN(1, 16384);
  DMA_X(7, 3);
  MMA(5, F2, xA, 0);
  SBAR();
  F2 = WFR(0, 24576);
  DMA_X(7, 4);
  MMA(6, F3, xA, 0);
  SBAR();
  F3 = WFR(1, 24576);
  DMA_X(7, 5);
  MMA(7, F0, xA, 0);
  SBAR();
  F0 = WFR(2, 24576);
  MM13(1, F1, xA, 0);
  SBAR();
  WAITV_BAR(8);
  F1 = WFR(3, 24576);
  DMA_K(1, 16384, 0);
  MMA(0, F2, xA, 1);
  SBAR();
  F2 = WFR_OWN(0, 24576);
  DMA_K(1, 16384, 1);
  MMA(1, F3, xA, 1);
  SBAR();
  F3 = WFR(4, 24576);
  MMA(2, F0, xA, 1);
  SBAR();
  F0 = WFR(5, 24576);
  MMA(3, F1, xA, 1);
  SBAR();
  F1 = WFR(6, 24576);
  MM13(0, F2, xA, 1);
  SBAR();
  F2 = WFR(7, 24576);
  MMA(4, F3, xA, 1);
  SBAR();
  F3 = WFR_OWN(1, 24576);
  MMA(5, F0, xA, 1);
  SBAR();
  F0 = WFR(0, 32768);
  WAITV(2);
  XRD(xB, 0, 1);
  XRD(xB, 1, 1);
  XRD(xB, 2, 1);
  XRD(xB, 3, 1);
  MMA(6, F1, xA, 1);
  SBAR();
  F1 = WFR(1, 32768);
  XRD(xB, 4, 1);
  XRD(xB, 5, 1);
  XRD(xB, 6, 1);
  XRD(xB, 7, 1);
  MMA(7, F2, xA, 1);
  SBAR();
  F2 = WFR(2, 32768);
  MM13(1, F3, xA, 1);
  SBAR();
  WAITV_BAR(8);
  F3 = WFR(3, 32768);
  DMA_K(2, 24576, 0);
  MMA(0, F0, xB, 0);
  SBAR();
  F0 = WFR_OWN(0, 32768);
  DMA_K(2, 24576, 1);
  MMA(1, F1, xB, 0);
  XPIN(xB);
  SBAR();
  F1 = WFR(4, 32768);
  MMA(2, F2, xB, 0);
  SBAR();
  F2 = WFR(5, 32768);
  MMA(3, F3, xB, 0);
  SBAR();
  F3 = WFR(6, 32768);
  MM13(0, F0, xB, 0);
  SBAR();
  F0 = WFR(7, 32768);
  MMA(4, F1, xB, 0);
  SBAR();
  F1 = WFR_OWN(1, 32768);
  MMA(5, F2, xB, 0);
  SBAR();
  F2 = WFR(0, 40960);
  MMA(6, F3, xB, 0);
  SBAR();
  F3 = WFR(1, 40960);
  MMA(7, F0, xB, 0);
  SBAR();
  F0 = WFR(2, 40960);
  MM13(1, F1, xB, 0);
  SBAR();
  BAR();
  F1 = WFR(3, 40960);
  DMA_V(1, 32768, 0);
  MMA(0, F2, xB, 1);
  SBAR();
  F2 = WFR_OWN(0, 40960);
  DMA_V(1, 32768, 1);
  MMA(1, F3, xB, 1);
  SBAR();
  F3 = WFR(4, 40960);
  DMA_K(3, 49152, 0);
  MMA(2, F0, xB, 1);
  SBAR();
  F0 = WFR(5, 40960);
  DMA_K(3, 49152, 1);
  MMA(3, F1, xB, 1);
  SBAR();
  F1 = WFR(6, 40960);
  MM13(0, F2, xB, 1);
  SBAR();
  F2 = WFR(7, 40960);
  MMA(4, F3, xB, 1);
  SBAR();
  F3 = WFR_OWN(1, 40960);
  MMA(5, F0, xB, 1);
  SBAR();
  MMA(6, F1, xB, 1);
  SBAR();
  MMA(7, F2, xB, 1);
  SBAR();
  MM13(1, F3, xB, 1);
  SBAR();
  BAR();
  DMA_V(2, 40960, 0);
  DMA_V(2, 40960, 1);
  auto mkq = [&](const f32x4& t0, const f32x4& t1) -> bf16x8 {
    unsigned a0 = cvtpk(t0[0] * QSCALE, t0[1] * QSCALE), a1 = cvtpk(t0[2] * QSCALE, t0[3] * QSCALE);
    unsigned b0 = cvtpk(t1[0] * QSCALE, t1[1] * QSCALE), b1 = cvtpk(t1[2] * QSCALE, t1[3] * QSCALE);
    auto r0 = __builtin_amdgcn_permlane32_swap(a0, b0, false, false); auto r1 = __builtin_amdgcn_permlane32_swap(a1, b1, false, false);
    auto s0 = __builtin_amdgcn_permlane16_swap(r0[0], r0[1], false, false); auto s1 = __builtin_amdgcn_permlane16_swap(r1[0], r1[1], false, false);
    u32x4 o = {s0[0], s1[0], s0[1], s1[1]};
    return __builtin_bit_cast(bf16x8, o);
  };
  s_.q13p = L + L_Q13 + lane * 16;
  s_.p13w = (lds_ptr)smem + L_P13 + lane * 16 + (w & 1) * 8; s_.p13w1 = s_.p13w + 1024;
  s_.act1 = (unsigned)__builtin_amdgcn_readfirstlane(w >> 1); s_.act0 = (unsigned)__builtin_amdgcn_readfirstlane((w >> 1) ^ 1);
  { const bf16x8 q13 = mkq(acc13[0], acc13[1]);
    *(__attribute__((address_space(3))) bf16x8*)((lds_ptr)s_.q13p + w * 1024) = q13; }
#pragma unroll
  for (int qb = 0; qb < 3; ++qb)
#pragma unroll
    for (int s = 0; s < 4; ++s) s_.Q[qb][s] = mkq(acc[qb][2 * s], acc[qb][2 * s + 1]);
  { const lds_cptr kp = L + ((q16 >> 2) * 2048 + (q16 & 3) * 64 + ((g ^ (2 * ((q16 >> 2) & 1))) * 16));
    const int o1 = (w & 1) * 256, o2 = 256 - o1;
    s_.kpA = kp + o1; s_.kpB = kp + o2; s_.vE1 = vE + o1; s_.vE2 = vE + o2; s_.vO1 = vO + o1; s_.vO2 = vO + o2; }
  WAITVL_BAR(8);

#pragma unroll
  for (int c = 0; c < 8; ++c) { s_.O[0][c] = f32x4{0, 0, 0, 0}; s_.O[1][c] = f32x4{0, 0, 0, 0}; s_.O[2][c] = f32x4{0, 0, 0, 0}; }
  s_.O13[0] = f32x4{0, 0, 0, 0}; s_.O13[1] = f32x4{0, 0, 0, 0};
  s_.ls[0] = s_.ls[1] = s_.ls[2] = 0.f; s_.l13 = 0.f;
  u32x4 PA[3], PB[3];
  {
    f32x4 T = {0, 0, 0, 0};
#pragma unroll
    for (int s = 0; s < 4; ++s) {
      const bf16x8 ka = KFR(0, 0, s), kb = KFR(0, 1, s), qf = lds_b128(s_.q13p + s * 1024);
#pragma unroll
      for (int qb = 0; qb < 3; ++qb) { s_.Sa[qb] = MFMA16(ka, s_.Q[qb][s], s == 0 ? (f32x4{0, 0, 0, 0}) : s_.Sa[qb]); s_.Sb[qb] = MFMA16(kb, s_.Q[qb][s], s == 0 ? (f32x4{0, 0, 0, 0}) : s_.Sb[qb]); }
      T = MFMA16(ka, qf, T);
    }
    { const float a = __builtin_amdgcn_exp2f(T[0]), b2 = __builtin_amdgcn_exp2f(T[1]), c = __builtin_amdgcn_exp2f(T[2]), d = __builtin_amdgcn_exp2f(T[3]);
      s_.l13 += (w < 2) ? (a + b2) + (c + d) : 0.f;
      *(__attribute__((address_space(3))) u32x2*)(s_.p13w) = u32x2{cvtpk(a, b2), cvtpk(c, d)}; }
    asm volatile("s_waitcnt lgkmcnt(0)" ::: "memory");
    s_.F0 = KFR(kslot(1), 0, 0); s_.F1 = KFR(kslot(1), 0, 1); s_.q0 = lds_b128(s_.q13p); s_.q1 = lds_b128(s_.q13p + 1024);
    SMH(s_.Sa, PA, 0, 0, 0); SMH(s_.Sa, PA, 0, 0, 1); SMH(s_.Sa, PA, 1, 0, 0); SMH(s_.Sa, PA, 1, 0, 1); SMH(s_.Sa, PA, 2, 0, 0); SMH(s_.Sa, PA, 2, 0, 1);
    SMH(s_.Sb, PA, 0, 1, 0); SMH(s_.Sb, PA, 0, 1, 1); SMH(s_.Sb, PA, 1, 1, 0); SMP(PA, 0); SMP(PA, 1); SMP(PA, 2);
    SBAR();
  }
  {
    const unsigned gw = (unsigned)bid * 4u + (unsigned)w;
    const char* wsrc = ((gw >> 10) ? woB : woA) + (size_t)(gw & 1023u) * 4096u; const unsigned vo_ = lane_id() * 16u;
    glds<0>(vo_, wsrc, lds0 + L_DUMP + (unsigned)w * 1024u); glds<0>(vo_, wsrc + 1024, lds0 + L_DUMP + (unsigned)w * 1024u);
    glds<0>(vo_, wsrc + 2048, lds0 + L_DUMP + (unsigned)w * 1024u); glds<0>(vo_, wsrc + 3072, lds0 + L_DUMP + (unsigned)w * 1024u);
  }
  for (int t = 0; t < 12; t += 4) {
    WAITV_BAR(6); att_iter<0, true, true, true, true>(s_, PA, PB, t);
    WAITV_BAR(6); att_iter<1, true, true, true, true>(s_, PB, PA, t + 1);
    WAITV_BAR(6); att_iter<2, true, true, true, true>(s_, PA, PB, t + 2);
    WAITV_BAR(6); att_iter<3, true, true, true, true>(s_, PB, PA, t + 3);
  }
  WAITV_BAR(6); att_iter<0, true, true, false, true>(s_, PA, PB, 12);
  WAITV_BAR(4); att_iter<1, true, true, false, false>(s_, PB, PA, 13);
  WAITV_BAR(2); att_iter<2, true, false, false, false>(s_, PA, PB, 14);
  *(__attribute__((address_space(3))) float*)((lds_ptr)smem + L_L13 + w * 256 + lane_id() * 4) = s_.l13;
  WAITVL_BAR(0); att_iter<3, false, false, false, false>(s_, PB, PA, 15);
  asm volatile("s_nop 7\n\ts_nop 1");
  SBAR();
  CS cs;
  const int half = idx & 1, ng = (xcd & 1) * 4 + ((idx >> 1) & 3);
  {
    const unsigned ln = lane_id();
    float l[4];
#pragma unroll
    for (int qb = 0; qb < 3; ++qb) { l[qb] = s_.ls[qb]; l[qb] += __shfl_xor(l[qb], 16); l[qb] += __shfl_xor(l[qb], 32); }
    l[3] = 0.f;
#pragma unroll
    for (int i = 0; i < 16; ++i) l[3] += *(const __attribute__((address_space(3))) float*)(L + L_L13 + i * 64 + (ln & 15) * 4);
    f32x4 chk = s_.O13[0] * 0.f + s_.O13[1] * 0.f;
#pragma unroll
    for (int qb = 0; qb < 3; ++qb)
#pragma unroll
      for (int c = 0; c < 8; ++c) chk += s_.O[qb][c] * 0.f;
    bool ok = (chk[0] + chk[1]) + (chk[2] + chk[3]) == 0.f;
#pragma unroll
    for (int qb = 0; qb < 4; ++qb) ok = ok && l[qb] >= 0x1p-30f && l[qb] <= 0x1p90f;
    if (FORCE_SLOW || __builtin_amdgcn_ballot_w64(!ok) != 0ull) att_slow(s_, l, lds0, w);
    char* obat = ws + WS_O + (size_t)(br * NB + b) * O_BATCH_BYTES;
    const __amdgpu_buffer_rsrc_t srd = __builtin_amdgcn_make_buffer_rsrc((void*)(obat + h * O_HEAD_BYTES), (short)0, (int)O_HEAD_BYTES, 0x00020000);
#pragma unroll
    for (int qb = 0; qb < 3; ++qb) {
      const float il = __builtin_amdgcn_rcpf(l[qb]);
#pragma unroll
      for (int s = 0; s < 4; ++s) {
        const f32x4 a = s_.O[qb][2 * s], c = s_.O[qb][2 * s + 1];
        u32x4 pk = {cvtpk(a[0] * il, a[1] * il), cvtpk(a[2] * il, a[3] * il), cvtpk(c[0] * il, c[1] * il), cvtpk(c[2] * il, c[3] * il)};
        __builtin_amdgcn_raw_buffer_store_b128(pk, srd, ln * 16 + (w * 3 + qb) * 4096 + s * 1024, 0, 16);
      }
    }
    { const float il = __builtin_amdgcn_rcpf(l[3]);
      const f32x4 a = s_.O13[0], c = s_.O13[1];
      u32x4 pk = {cvtpk(a[0] * il, a[1] * il), cvtpk(a[2] * il, a[3] * il), cvtpk(c[0] * il, c[1] * il), cvtpk(c[2] * il, c[3] * il)};
      __builtin_amdgcn_raw_buffer_store_b128(pk, srd, ln * 16 + 12 * 4096 + w * 1024, 0, 16); }
    cs.tag_lo = (unsigned)tag; cs.tag_hi = (unsigned)(tag >> 32); cs.flags = ws + WS_FLAGS + (br * NB + b) * 256;
    if (bid < 256) {
      const unsigned gg = ln >> 4, qq = (ln & 15) >> 2, pp = ln & 3, rr = (ln >> 2) & 7;
      cs.srdO = __builtin_amdgcn_make_buffer_rsrc((void*)(obat + half * (7 * 4096)), (short)0, (int)O_BATCH_BYTES - half * (7 * 4096), 0x00020000);
      cs.srdW = __builtin_amdgcn_make_buffer_rsrc((void*)((br ? woB : woA) + ng * 128), (short)0, 4096 * 1024 - 896, 0x00020000);
      cs.voffO = ln * 16; cs.voffW = rr * 1024 + (4 * (ln >> 5) + (pp ^ (2 * (rr >> 2)))) * 16;
      cs.wdst = lds0 + (unsigned)w * C_WAVE; cs.sw = (unsigned)w * 1024u;
      cs.aE = L + (w * C_WAVE + (gg >> 1) * 1024 + (4 * (gg & 1) + qq) * 64 + (gg & 1) * 32 + 8 * pp);
      cs.aO = L + (w * C_WAVE + (gg >> 1) * 1024 + (4 * (gg & 1) + qq) * 64 + ((gg & 1) ^ 1) * 32 + 8 * pp);
      cs.ready = 1u << h; cs.hx = (unsigned)(xcd & 1) << 4; cs.fo = (ln & 31) * 8;
      WAITL_BAR();
#pragma unroll
      for (int s = 0; s < 4; ++s)
#pragma unroll
        for (int j = 0; j < 4; ++j) C_LDW(cs.wdst + s * C_SLOT + j * 1024, c_head((unsigned)s) * 131072u + cs.sw * 32u + j * 8192u);
    }
    WAITV_BAR(0);
    if (w == 0) {
      const u32x2 tv = {cs.tag_lo, cs.tag_hi}; const unsigned zero = 0u;
      const char* fl = cs.flags + h * 8;
      asm volatile("s_mov_b64 exec, 1\n\tglobal_store_dwordx2 %0, %1, %2 sc1\n\ts_mov_b64 exec, -1" :: "v"(zero), "v"(tv), "s"(fl) : "memory");
    }
  }
  if (bid < 256) {
    __builtin_amdgcn_s_setprio(0);
    char* outp = out + (size_t)(b * SQ + half * 112) * 2048 + br * 1024 + ng * 128;
    char* epoch = (half == 0 && ng == 0) ? ws + WS_EPOCH + (br * NB + b) * 256 : nullptr;
    if (half == 0) phase_c<7>(cs, outp, epoch, smem, w); else phase_c<6>(cs, outp, epoch, smem, w);
  }
}

extern "C" void kernel_launch(void* const* d_in, const int* in_sizes, int n_in, void* d_out, int out_size, void* d_ws, size_t ws_size, hipStream_t stream) {
  (void)hipFuncSetAttribute((const void*)mha2_kernel, hipFuncAttributeMaxDynamicSharedMemorySize, L_ATT);
  mha2_kernel<<<512, 256, L_ATT, stream>>>((const char*)d_in[0], (const char*)d_in[1], (const char*)d_in[2], (const char*)d_in[3], (const char*)d_in[4],
                                           (const char*)d_in[5], (const char*)d_in[6], (const char*)d_in[7], (const char*)d_in[8], (const char*)d_in[9], (char*)d_ws, (char*)d_out);
}
```
